```python
import torch

def _softmax(x, axis=-1):
    return torch.softmax(x, dim=axis)

def _gelu(x):
    return torch.nn.functional.gelu(x, approximate='tanh')

def _silu(x):
    return torch.nn.functional.silu(x)

def _sigmoid(x):
    return torch.sigmoid(x)

def _layernorm(x, axis=-1, eps=1e-05):
    m = x.mean(dim=axis, keepdim=True)
    v = x.var(dim=axis, keepdim=True, unbiased=False)
    return (x - m) / torch.sqrt(v + eps)

def _scatter_add(tgt, idx, src, axis=0):
    return tgt.clone().index_add_(axis, idx, src)

def _segment_sum(src, seg, n):
    out = torch.zeros((n,) + src.shape[1:], dtype=src.dtype)
    return out.index_add_(0, seg, src)

def _pad(x, pw):
    flat = tuple((v for pair in reversed(pw) for v in pair))
    return torch.nn.functional.pad(x, flat)
kernel_name = 'parallel_sum__memory_aug_recurrent_mem_tr__mixer_gmlp__prompt'

def setup_inputs():

    _w = lambda a: (lambda t: t.to(torch.bfloat16) if t.is_floating_point() else t)(torch.as_tensor(a))

    def _A_setup():
        B, S, Dm = (1, 2048, 512)
        H, Dh = (32, 128)
        M, seg = (8, 32)
        seg = min(seg, S)
        S = S // seg * seg
        return {'x': _w((lambda *s: torch.randn(*s, dtype=torch.bfloat16) * 0.05)(B, S, Dm)), 'mem0': _w((lambda *s: torch.randn(*s, dtype=torch.bfloat16) * 1.6)(B, M, Dm)), 'Wq': _w((lambda *s: torch.randn(*s, dtype=torch.bfloat16) * 0.8)(Dm, H, Dh) * Dm ** (-0.5)), 'Wk': _w((lambda *s: torch.randn(*s, dtype=torch.bfloat16) * 0.8)(Dm, H, Dh) * Dm ** (-0.5)), 'Wv': _w((lambda *s: torch.randn(*s, dtype=torch.bfloat16) * 1.6)(Dm, H, Dh) * Dm ** (-0.5)), 'Wo': _w((lambda *s: torch.randn(*s, dtype=torch.bfloat16) * 1.6)(H, Dh, Dm) * (H * Dh) ** (-0.5)), 'seg_len': seg}
    _A_in = _A_setup()

    def _B_setup():
        B, S, D, F = (1, 2048, 512, 1792)
        Fh = F // 2
        return {'x': _w((lambda *s: torch.randn(*s, dtype=torch.bfloat16) * 0.05)(B, S, D)), 'w_in': _w((lambda *s: torch.randn(*s, dtype=torch.bfloat16) * 0.2)(D, 2 * Fh)), 'w_sp': _w(0.01 * (lambda *s: torch.randn(*s, dtype=torch.bfloat16) * 0.2)(S, S)), 'b_sp': _w(1.0 + 0.01 * (lambda *s: torch.randn(*s, dtype=torch.bfloat16) * 0.05)(S)), 'w_out': _w((lambda *s: torch.randn(*s, dtype=torch.bfloat16) * 0.05)(Fh, D))}
    _B_in = _B_setup()
    return {'A_x': _A_in['x'], 'A_mem0': _A_in['mem0'], 'A_Wq': _A_in['Wq'], 'A_Wk': _A_in['Wk'], 'A_Wv': _A_in['Wv'], 'A_Wo': _A_in['Wo'], 'A_seg_len': _A_in['seg_len'], 'B_x': _B_in['x'], 'B_w_in': _B_in['w_in'], 'B_w_sp': _B_in['w_sp'], 'B_b_sp': _B_in['b_sp'], 'B_w_out': _B_in['w_out']}

def reference(A_x, A_mem0, A_Wq, A_Wk, A_Wv, A_Wo, A_seg_len, B_x, B_w_in, B_w_sp, B_b_sp, B_w_out):

    def _A_ref(x, mem0, Wq, Wk, Wv, Wo, seg_len):
        B, S, Dm = x.shape
        H, Dh = (Wq.shape[1], Wq.shape[2])
        M = mem0.shape[1]
        scale = Dh ** (-0.5)
        n_seg = S // seg_len
        L = M + seg_len
        idx = torch.arange(L)
        mask = (idx[:, None] < M) | (idx[:, None] >= idx[None, :])
        mem = mem0
        outs = []
        for s in range(n_seg):
            xs = x[:, s * seg_len:(s + 1) * seg_len, :].clone()
            h = torch.concatenate([mem, xs], axis=1)
            q = torch.einsum('btd,dhe->bhte', h, Wq)
            k = torch.einsum('btd,dhe->bhte', h, Wk)
            v = torch.einsum('btd,dhe->bhte', h, Wv)
            scores = torch.einsum('bhqe,bhke->bhqk', q, k) * scale
            scores = torch.where(mask[None, None], scores, -1000000000.0)
            p = _softmax(scores, axis=-1)
            o = torch.einsum('bhqk,bhke->bhqe', p, v)
            o = torch.einsum('bhte,hed->btd', o, Wo)
            mem = o[:, :M, :].clone()
            outs.append(o[:, M:, :])
        return torch.concatenate(outs, axis=1)

    def _B_ref(x, w_in, w_sp, b_sp, w_out):
        B, S, D = x.shape
        F = w_in.shape[-1]
        Fh = F // 2
        z = _gelu(torch.einsum('bsd,df->bsf', x, w_in))
        z1 = z[..., :Fh].clone()
        z2 = _layernorm(z[..., Fh:], axis=-1)
        g = torch.einsum('ts,bsf->btf', w_sp, z2) + b_sp[None, :, None]
        return torch.einsum('bsf,fd->bsd', z1 * g, w_out)
    x = A_x
    xn = _layernorm(x, axis=-1)
    return x + _A_ref(xn, A_mem0, A_Wq, A_Wk, A_Wv, A_Wo, A_seg_len) + _B_ref(xn, B_w_in, B_w_sp, B_b_sp, B_w_out)
```

```cpp
#include <hip/hip_runtime.h>
#include <hip/hip_bf16.h>
#pragma clang diagnostic ignored "-Winline-asm"

#define LAS __attribute__((address_space(3)))
#define GAS __attribute__((address_space(1)))
typedef __bf16 bf4 __attribute__((ext_vector_type(4)));
typedef __bf16 bf8 __attribute__((ext_vector_type(8)));
typedef __bf16 bf2 __attribute__((ext_vector_type(2)));
typedef float f32x4 __attribute__((ext_vector_type(4)));
typedef unsigned u32x2 __attribute__((ext_vector_type(2)));
typedef unsigned u32x4 __attribute__((ext_vector_type(4)));
typedef unsigned u32x8 __attribute__((ext_vector_type(8)));
typedef unsigned u32x16 __attribute__((ext_vector_type(16)));
typedef unsigned short u16;
typedef __amdgpu_buffer_rsrc_t rsrc_t;

constexpr int MB_ = 1 << 20;
constexpr int WS_MA = 0;
constexpr int WS_PB = 66 * 8192;
constexpr int WS_FL = 0x1A0000;
constexpr int WS_INIT_END = 0x1B0000;
constexpr int WS_TOK = 0x1B0000, WS_SELW = 0x1B0200, WS_KV0 = 0x1B2000, WS_DONE = 0x1B1000;
constexpr int WS_PROBE = 0x1C0000;
constexpr int FL_KVF = WS_FL;
constexpr int FL_PF = WS_FL + 0x1000;
constexpr int FL_ZC = WS_FL + 0x4000;
constexpr int FL_UC = WS_FL + 0x5000;
constexpr int FL_ZN = WS_FL + 0x6000;
constexpr int FL_ALL = WS_FL + 0x6100;
constexpr int FL_VOTE = WS_FL + 0x6200;
constexpr int FL_HEAD = WS_FL + 0x7000;
constexpr int FL_XC = WS_FL + 0x8000;
constexpr int WS_XN = 2 * MB_;
constexpr int WS_KX = 4 * MB_, WS_VX = 20 * MB_, WS_QX = 36 * MB_;
constexpr int WS_KM = 53 * MB_, WS_VM = 62 * MB_;
constexpr int SL = 60;
constexpr int WS_ST = 79 * MB_;
constexpr int WS_Z = 72 * MB_, WS_Z2N = 80 * MB_, WS_U = 84 * MB_, WS_BASE = 88 * MB_, WS_XS = 96 * MB_;
constexpr int WS_END = 128 * MB_;
constexpr int LDS_BYTES = 160 * 1024;
constexpr int SPIN_MAX = 400000;
#define RSLEEP 8
#define CH_DA (5 + 256 * 7 + 65536 * 600)
#define CH_DB (1 + 256 * 34)

__host__ __device__ __forceinline__ int remap(int off, int sel) { return (off & 8191) | ((off >> 13) << 14) | (sel << 13); }
__device__ __forceinline__ float u2f(unsigned u) { return __builtin_bit_cast(float, u); }
__device__ __forceinline__ unsigned f2u(float f) { return __builtin_bit_cast(unsigned, f); }
__device__ __forceinline__ unsigned pk_bf16(float a, float b) { bf2 v; v[0] = (__bf16)a; v[1] = (__bf16)b; return __builtin_bit_cast(unsigned, v); }
__device__ __forceinline__ float rbf(float a) { return (float)(__bf16)a; }
__device__ __forceinline__ float blo(unsigned u) { return u2f(u << 16); }
__device__ __forceinline__ float bhi(unsigned u) { return u2f(u & 0xFFFF0000u); }
__device__ __forceinline__ f32x4 mf32(bf8 a, bf8 b, f32x4 c) { return __builtin_amdgcn_mfma_f32_16x16x32_bf16(a, b, c, 0, 0, 0); }
__device__ __forceinline__ f32x4 mf32u(u32x4 a, u32x4 b, f32x4 c) { return mf32(__builtin_bit_cast(bf8, a), __builtin_bit_cast(bf8, b), c); }
__device__ __forceinline__ bf4 trread(char* smem, int off) { return __builtin_amdgcn_ds_read_tr16_b64_v4bf16((LAS bf4*)(smem + off)); }
__device__ __forceinline__ rsrc_t mkrs(const void* p, int bytes) { return __builtin_amdgcn_make_buffer_rsrc((void*)p, 0, bytes, 0x00020000); }
__device__ __forceinline__ u32x4 ld16(rsrc_t r, int off) { return __builtin_amdgcn_raw_buffer_load_b128(r, off, 0, 0); }
__device__ __forceinline__ u32x4 ld16s(rsrc_t r, int off) { return __builtin_amdgcn_raw_buffer_load_b128(r, off, 0, 16); }
__device__ __forceinline__ void st16s(rsrc_t r, int off, u32x4 v) { __builtin_amdgcn_raw_buffer_store_b128(v, r, off, 0, 16); }
__device__ __forceinline__ float xmax32(float a) { auto r = __builtin_amdgcn_permlane32_swap(f2u(a), f2u(a), false, false); return fmaxf(u2f(r[0]), u2f(r[1])); }
__device__ __forceinline__ float xmax16(float a) { auto r = __builtin_amdgcn_permlane16_swap(f2u(a), f2u(a), false, false); return fmaxf(u2f(r[0]), u2f(r[1])); }
__device__ __forceinline__ float xsum32(float a) { auto r = __builtin_amdgcn_permlane32_swap(f2u(a), f2u(a), false, false); return u2f(r[0]) + u2f(r[1]); }
__device__ __forceinline__ float xsum16(float a) { auto r = __builtin_amdgcn_permlane16_swap(f2u(a), f2u(a), false, false); return u2f(r[0]) + u2f(r[1]); }
template <int CTRL> __device__ __forceinline__ float xdpp(float a) { return a + u2f(__builtin_amdgcn_update_dpp(0, f2u(a), CTRL, 0xf, 0xf, true)); }
__device__ __forceinline__ float wsum(float a) {
  a = xdpp<0xB1>(a); a = xdpp<0x4E>(a); a = xdpp<0x124>(a); a = xdpp<0x128>(a);
  return xsum32(xsum16(a));
}
#define RLX_AGENT __ATOMIC_RELAXED, __HIP_MEMORY_SCOPE_AGENT
#define BAR() do { asm volatile("s_waitcnt lgkmcnt(0)" ::: "memory"); __builtin_amdgcn_s_barrier(); asm volatile("" ::: "memory"); } while (0)
#define DRAIN() asm volatile("s_waitcnt vmcnt(0)" ::: "memory")

__device__ __forceinline__ void signal_add(char* ws, int off) {
  DRAIN(); BAR();
  if (threadIdx.x == 0) __hip_atomic_fetch_add((GAS int*)(ws + off), 1, RLX_AGENT);
}
__device__ __forceinline__ void poll_ge(char* ws, int off, int want) {
  _Pragma("unroll 1") for (int sp = 0; sp < SPIN_MAX; ++sp) {
    if (__builtin_amdgcn_readfirstlane(__hip_atomic_load((GAS int*)(ws + off), RLX_AGENT)) >= want) return;
    __builtin_amdgcn_s_sleep(4);
  }
}
__device__ __forceinline__ void wait_acquire(char* ws, int off, int want) {
  if (__builtin_amdgcn_readfirstlane(threadIdx.x) < 64) {
    poll_ge(ws, off, want);
    __builtin_amdgcn_fence(__ATOMIC_ACQUIRE, "agent");
    DRAIN();
  }
  BAR();
}

template <bool SC1 = false>
__device__ __forceinline__ void dma16(rsrc_t r, int ldsw, int ldsc, int voff, int soff) {
  int t;
  if (SC1)
    asm volatile("s_add_u32 m0, %1, %2\n\ts_mov_b32 %0, %5\n\tbuffer_load_dwordx4 %3, %4, %0 offen sc1 lds"
                 : "=&s"(t) : "s"(ldsw), "i"(ldsc), "v"(voff), "s"(r), "i"(soff) : "m0", "scc");
  else
    asm volatile("s_add_u32 m0, %1, %2\n\ts_mov_b32 %0, %5\n\tbuffer_load_dwordx4 %3, %4, %0 offen lds"
                 : "=&s"(t) : "s"(ldsw), "i"(ldsc), "v"(voff), "s"(r), "i"(soff) : "m0", "scc");
}
template <int NH> struct Geo { static constexpr int NT = 4 * NH, STAGE = 16384 * (1 + NH), LOADS = 4 * (1 + NH), D = NH == 2 ? 3 : 5; };
template <int V> struct IC { static constexpr int value = V; };
template <int A, int B, class F> __device__ __forceinline__ void static_for(F&& f) { if constexpr (A < B) { f(IC<A>{}); static_for<A + 1, B>(f); } }
template <int N> __device__ __forceinline__ void wait_vm() { asm volatile("s_waitcnt vmcnt(%0)" :: "i"(N) : "memory"); }

template <int NH, bool PERM, bool SW, int nt, int AT, int AI, int AJ, int BT, int BI, bool AEARLY, class H>
__device__ __forceinline__ void gemm_loop(H&& hook, char* smem, rsrc_t ra, int avo, rsrc_t rb0, rsrc_t rb1, int bvo0, int bvo1, int bxor,
                                          f32x4 (&acc)[4][4 * NH], int w, int l) {
  using G = Geo<NH>;
  const int lr = l & 15, lg = l >> 4, q4 = (l >> 2) & 3, p4 = l & 3, wm = w >> 1, wn = w & 1;
  int aoff[2], boff[G::NT];
#pragma unroll
  for (int kk = 0; kk < 2; ++kk) aoff[kk] = (64 * wm + lr) * 128 + (((4 * kk + lg) ^ (lr & 7)) << 4);
  const int X = (q4 | ((lg & 1) << 2)) << 1;
#pragma unroll
  for (int j = 0; j < G::NT; ++j) {
    const int jj = (NH == 1 ? 4 * wn : 0) + j;
    const int ch = PERM ? 4 * (jj >> 1) + p4 : 2 * jj + (p4 >> 1), hf = PERM ? (jj & 1) : (p4 & 1);
    boff[j] = 16384 + (NH == 2 ? wn * 16384 : 0) + (8 * lg + q4) * 256 + ((ch ^ X) << 4) + hf * 8;
  }
  const int bx0 = bvo0 ^ bxor, bx1 = bvo1 ^ bxor;
  const int w4 = __builtin_amdgcn_readfirstlane(w << 12);
  auto piece = [&](int t, int i) __attribute__((always_inline)) {
    const int base = (t % G::D) * G::STAGE + (i >> 2) * 16384 + (i & 3) * 1024;
    if (i < 4) dma16<NH == 2>(ra, w4, base, avo, t * AT + (i >> 1) * AI + (i & 1) * (AJ));
    else if (i < 8) dma16(rb0, w4, base, (i & 2) ? bx0 : bvo0, t * BT + (i & 3) * BI);
    else dma16(rb1, w4, base, (i & 2) ? bx1 : bvo1, t * BT + (i & 3) * BI);
  };
  bf8 fa[2][4], fb[2][G::NT];
  auto rdb = [&](int s, int t, int kk, int j) __attribute__((always_inline)) {
    char* sb = smem + (t % G::D) * G::STAGE;
    if (j < 4) fa[s][j] = *(const bf8*)(sb + aoff[kk] + j * 2048);
    const bf4 b0 = trread(sb, boff[j] + kk * 8192), b1 = trread(sb, boff[j] + kk * 8192 + 1024);
    fb[s][j] = __builtin_shufflevector(b0, b1, 0, 1, 2, 3, 4, 5, 6, 7);
  };
  auto half = [&](int s, int tn, int kn, int td, int p0) __attribute__((always_inline)) {
#pragma unroll
    for (int j = 0; j < G::NT; ++j) {
      if (tn < nt) rdb(s ^ 1, tn, kn, j);
      acc[0][j] = SW ? mf32(fb[s][j], fa[s][0], acc[0][j]) : mf32(fa[s][0], fb[s][j], acc[0][j]);
      __builtin_amdgcn_sched_barrier(0);
      if (td >= G::D && td < nt && j < G::LOADS / 2) piece(td, p0 + j);
#pragma unroll
      for (int i = 1; i < 4; ++i) acc[i][j] = SW ? mf32(fb[s][j], fa[s][i], acc[i][j]) : mf32(fa[s][i], fb[s][j], acc[i][j]);
      __builtin_amdgcn_sched_barrier(0);
    }
  };
#pragma unroll
  for (int i = 0; i < 4; ++i)
#pragma unroll
    for (int j = 0; j < G::NT; ++j) acc[i][j] = (f32x4){0, 0, 0, 0};
  constexpr int NL = AEARLY ? G::LOADS - 4 : 4;
  constexpr int EN = NH == 2 ? 1 : G::D;
  auto early = [&](int t) __attribute__((always_inline)) {
#pragma unroll
    for (int i = AEARLY ? 0 : 4; i < (AEARLY ? 4 : G::LOADS); ++i) piece(t, i);
  };
  BAR();
  if (!hook(0)) {
#pragma unroll
    for (int t = 0; t < EN; ++t) early(t);
  }
  hook(1);
#pragma unroll
  for (int t = 0; t < G::D; ++t) {
    if (t >= EN) early(t);
#pragma unroll
    for (int i = AEARLY ? 4 : 0; i < (AEARLY ? G::LOADS : 4); ++i) piece(t, i);
  }
  constexpr int PP = EN == 1 ? G::LOADS : NL;
  wait_vm<PP * (G::D - 1)>();
  BAR();
#pragma unroll
  for (int j = 0; j < G::NT; ++j) rdb(0, 0, 0, j);
  __builtin_amdgcn_sched_barrier(0);
  static_for<0, nt>([&](auto tc) __attribute__((always_inline)) {
    constexpr int t = decltype(tc)::value;
    half(0, t, 1, t + G::D - 1, G::LOADS / 2);
    if constexpr (t + 1 < nt) {
      constexpr int hi = t + G::D - 1 < nt - 1 ? t + G::D - 1 : nt - 1;
      constexpr int npro = G::D - 1 - (t + 1) > 0 ? G::D - 1 - (t + 1) : 0;
      wait_vm<PP * npro + G::LOADS * (hi - (t + 1) - npro)>();
      BAR();
    }
    half(1, t + 1, 0, t + G::D, 0);
  });
}
__device__ __forceinline__ int a_voff(int w, int l, int lda2) { const int r = 32 * w + (l >> 3); return r * lda2 + (((l & 7) ^ (r & 7)) << 4); }
__device__ __forceinline__ int b_voff(int w, int l, int ldb2) {
  return (16 * w + (l >> 4)) * ldb2 + (((l & 15) ^ ((l >> 4) << 1)) << 4);
}

struct Args {
  const u16 *x, *mem0, *Wq, *Wk, *Wv, *Wo, *w_in, *w_sp, *b_sp, *w_out;
  u16* out;
  char* ws;
  int da, db;
};

__device__ __forceinline__ void store_kstyle(rsrc_t rw, int base, f32x4 (&acc)[4][8], int l) {
#pragma unroll
  for (int i = 0; i < 4; ++i)
#pragma unroll
    for (int u = 0; u < 4; ++u) {
      const f32x4 a = acc[i][2 * u], b = acc[i][2 * u + 1];
      const u32x4 o = {pk_bf16(a[0], a[1]), pk_bf16(a[2], a[3]), pk_bf16(b[0], b[1]), pk_bf16(b[2], b[3])};
      st16s(rw, base + (i >> 1) * (32 * 8192) + ((i & 1) * 4 + u) * 1024 + l * 16, o);
    }
}

__device__ __forceinline__ float gelu_t(float x) {
  const float e = __builtin_amdgcn_exp2f(x * __builtin_fmaf(x * x, -0.10294323970f, -2.30220819814f));
  return x * __builtin_amdgcn_rcpf(1.0f + e);
}

#define LANE_VARS \
  int tid = threadIdx.x; asm volatile("" : "+v"(tid)); \
  int bid = blockIdx.x; asm volatile("" : "+s"(bid)); \
  const int l = tid & 63, w = __builtin_amdgcn_readfirstlane(tid >> 6); \
  const int lr = l & 15, lg = l >> 4, wm = w >> 1, wn = w & 1; \
  const int xg = (bid & 7) - 2, idx = bid >> 3; \
  (void)lr; (void)lg; (void)wm; (void)wn; (void)idx;

__device__ __forceinline__ void bulk(const Args& p, char* smem) {
  typedef unsigned long long u64;
  char* ws = p.ws;
  const rsrc_t rw = mkrs(ws, WS_END);
  auto tok = [&]() __attribute__((always_inline)) { return __hip_atomic_load((GAS u64*)(ws + WS_TOK), RLX_AGENT) + 1; };
  const u64 tstart = __builtin_amdgcn_s_memrealtime();
  {
    LANE_VARS
    const int nh = xg < 4 ? 5 : 6, h0 = xg < 4 ? 5 * xg : 6 * xg - 4;
  auto wait_all = [&]() __attribute__((always_inline)) {
    const u64 token = tok();
    _Pragma("unroll 1") for (int sp = 0; sp < SPIN_MAX; ++sp) {
      bool ok = true;
#pragma unroll
      for (int i = 0; i < 3; ++i) ok &= __hip_atomic_load((GAS u64*)(ws + WS_DONE + 256 + min(i * 64 + l, 159) * 8), RLX_AGENT) == token;
      if (__all(ok)) break;
      __builtin_amdgcn_s_sleep(4);
    }
  };
    if (idx < nh) {
      const rsrc_t rk = mkrs(p.Wk, 4 * MB_), rv = mkrs(p.Wv, 4 * MB_);
      const int bvo = (h0 + idx) * 256 + b_voff(w, l, 8192), w4 = __builtin_amdgcn_readfirstlane(w << 12);
#pragma unroll
      for (int i = 4; i < 12; ++i) dma16(i < 8 ? rk : rv, w4, (i >> 2) * 16384 + (i & 3) * 1024, (i & 2) ? bvo ^ 128 : bvo, (i & 3) * 4 * 8192);
      if (w == 0) {
        const u64 token = tok();
        _Pragma("unroll 1") for (int sp = 0; sp < SPIN_MAX; ++sp) {
          if (__all(__hip_atomic_load((GAS u64*)(ws + WS_DONE + (l & 31) * 8), RLX_AGENT) == token)) break;
          __builtin_amdgcn_s_sleep(1);
        }
      }
    } else {
    const int b = xg * 32 - h0 + idx - nh, r0 = b * 4 + w;
    u32x4 xr[4];
#pragma unroll
    for (int i = 0; i < 4; ++i) xr[i] = *(const u32x4*)(p.x + min(r0 + 640 * i, 2047) * 512 + l * 8);
    const u64 token = tok();
#pragma unroll
    for (int i = 0; i < 4; ++i) {
      float v[8], sum = 0, q = 0;
#pragma unroll
      for (int j = 0; j < 4; ++j) { v[2 * j] = blo(xr[i][j]); v[2 * j + 1] = bhi(xr[i][j]); sum += v[2 * j] + v[2 * j + 1]; }
      sum = wsum(sum);
      const float mean = sum * (1.0f / 512.0f);
#pragma unroll
      for (int j = 0; j < 8; ++j) q += (v[j] - mean) * (v[j] - mean);
      q = wsum(q);
      const float mb = rbf(mean), rs = 1.0f / rbf(sqrtf(rbf(rbf(q * (1.0f / 512.0f)) + 1e-5f)));
      u32x4 o;
#pragma unroll
      for (int j = 0; j < 4; ++j) o[j] = pk_bf16(rbf(v[2 * j] - mb) * rs, rbf(v[2 * j + 1] - mb) * rs);
      st16s(rw, r0 + 640 * i < 2048 ? WS_XN + (r0 + 640 * i) * 1024 + l * 16 : WS_END, o);
      if (i == 0 && b < 32) {
        DRAIN(); BAR();
        if (tid == 0) __hip_atomic_store((GAS u64*)(ws + WS_DONE + b * 8), token, RLX_AGENT);
      }
    }
    const u32x4 ff = {~0u, ~0u, ~0u, ~0u};
#pragma unroll
    for (int i = 0; i < 3; ++i) { const int o = (b * 256 + tid + i * 40960) * 16; st16s(rw, o < WS_INIT_END ? o : WS_END, ff); }
    DRAIN(); BAR();
    if (tid == 0) __hip_atomic_store((GAS u64*)(ws + WS_DONE + 256 + b * 8), token, RLX_AGENT);
    int vote = 0;
    if (idx == 31) {
      GAS unsigned* pp = (GAS unsigned*)(ws + WS_PROBE + xg * 512 + w * 128 + (l & 31) * 4);
#pragma unroll
      for (int i = 0; i < 12; ++i) __hip_atomic_store(pp + i * 1024, (unsigned)i, RLX_AGENT);
      DRAIN();
      unsigned bt[2] = {~0u, ~0u};
#pragma unroll 1
      for (int i = 0; i < 6; ++i) {
#pragma unroll
        for (int c = 0; c < 2; ++c) {
          __builtin_amdgcn_sched_barrier(0);
          const unsigned t0 = (unsigned)__builtin_amdgcn_s_memtime();
          asm volatile("s_waitcnt lgkmcnt(0)" ::: "memory");
          unsigned v = __hip_atomic_load(pp + (c * 2 + (i & 1) + (i >> 1) * 4) * 1024, RLX_AGENT);
          asm volatile("s_waitcnt vmcnt(0)" : "+v"(v) :: "memory");
          bt[c] = min(bt[c], (unsigned)__builtin_amdgcn_s_memtime() - t0);
        }
      }
      if (l == 0) { *(volatile LAS unsigned*)(smem + w * 8) = bt[0]; *(volatile LAS unsigned*)(smem + w * 8 + 4) = bt[1]; }
      BAR();
      const volatile LAS unsigned* bb = (const volatile LAS unsigned*)smem;
      const int d = (int)min(min(bb[0], bb[2]), min(bb[4], bb[6])) - (int)min(min(bb[1], bb[3]), min(bb[5], bb[7]));
      vote = d > 96 ? 1 : d < -96 ? -1 : 0;
      const int xcc = __builtin_amdgcn_s_getreg(20 | (3 << 11)) & 7;
      if ((xcc ^ (xcc - 2 - xg)) & 4) vote = -vote;
    }
    if (w == 0) {
      if (idx != 31) { while (__builtin_amdgcn_s_memrealtime() - tstart < (u64)(p.da >> 16)) __builtin_amdgcn_s_sleep(8); }
      wait_all();
      if (idx == 31 && l == 0) {
        const int tot = __hip_atomic_fetch_add((GAS int*)(ws + FL_VOTE), vote + 16, RLX_AGENT) + 1 + vote + 16;
        if (tot >= 90) __hip_atomic_store((GAS u64*)(ws + WS_SELW), token ^ (tot > 96 ? (1ull << 45) : 0ull), RLX_AGENT);
      }
    }
    BAR();
    }
  }
  int e = blockIdx.x >> 3;
#pragma unroll 1
  for (;;) {
    LANE_VARS
    const rsrc_t rxn = mkrs(ws + WS_XN, 2 * MB_);
  const int nh = xg < 4 ? 5 : 6, h0 = xg < 4 ? 5 * xg : 20 + 6 * (xg - 4);
  const int nKV = 16 * nh;
  const int q0 = xg < 4 ? 48 * xg : 192 + 32 * (xg - 4), nQ = xg < 4 ? 48 : 32;
  const int n0 = (64 * xg) / 6, nN = (64 * (xg + 1)) / 6 - n0;
  const int s0 = (112 * xg) / 6, nS = (112 * (xg + 1)) / 6 - s0;
  const int z0 = 4 * ((28 * xg) / 6), nZ = 4 * ((28 * (xg + 1)) / 6) - z0;
  const int e1 = nh, e2 = e1 + nZ, e3 = e2 + 3 * nh, e4 = e3, e5 = e4 + nS, e6 = e5 + nKV - 4 * nh, e7 = e6 + nN, e8 = e7 + nQ;
  auto wait_all = [&]() __attribute__((always_inline)) {
    const u64 token = tok();
    _Pragma("unroll 1") for (int sp = 0; sp < SPIN_MAX; ++sp) {
      bool ok = true;
#pragma unroll
      for (int i = 0; i < 3; ++i) ok &= __hip_atomic_load((GAS u64*)(ws + WS_DONE + 256 + min(i * 64 + l, 159) * 8), RLX_AGENT) == token;
      if (__all(ok)) break;
      __builtin_amdgcn_s_sleep(4);
    }
  };
    if (e >= e8) break;
    if (e < e1 || (e >= e2 && e < e3) || (e >= e5 && e < e6)) {
      const rsrc_t rk = mkrs(p.Wk, 4 * MB_), rv = mkrs(p.Wv, 4 * MB_);
      const int i = e < e1 ? e : e < e3 ? e - nZ : e - e5 + 4 * nh, mb = i / nh, h = h0 + i % nh;
      f32x4 acc[4][8];
      const int avo = mb * 128 * 1024 + a_voff(w, l, 1024), bvo = h * 256 + b_voff(w, l, 8192);
      const int ob = ((mb * 4 + 2 * wm) * 32 + h) * 8192;
      if (wn == 0) {
        gemm_loop<2, false, true, 8, 128, 16384, 8192, 64 * 8192, 4 * 8192, false>([&](int) { return e < e1; }, smem, rxn, avo, rk, rv, bvo, bvo, 128, acc, w, l);
        store_kstyle(rw, WS_KX + ob, acc, l);
      } else {
        gemm_loop<2, false, false, 8, 128, 16384, 8192, 64 * 8192, 4 * 8192, false>([&](int) { return e < e1; }, smem, rxn, avo, rk, rv, bvo, bvo, 128, acc, w, l);
#pragma unroll
        for (int sg = 0; sg < 2; ++sg)
#pragma unroll
          for (int j = 0; j < 8; ++j) {
            const f32x4 a = acc[2 * sg][j], b = acc[2 * sg + 1][j];
            const u32x4 o = {pk_bf16(a[0], a[1]), pk_bf16(a[2], a[3]), pk_bf16(b[0], b[1]), pk_bf16(b[2], b[3])};
            st16s(rw, WS_VX + ob + sg * (32 * 8192) + j * 1024 + l * 16, o);
          }
      }
      if (mb == 15) {
#pragma unroll
        for (int k = 0; k < 8; ++k) st16s(rw, ((k & 1) ? WS_VM : WS_KM) + ((SL + (k >> 1)) * 32 + h) * 4096 + tid * 16, (u32x4){~0u, ~0u, ~0u, ~0u});
      }
      if (e < e1) {
        const u64 token = tok();
        DRAIN(); BAR();
        if (tid == 0) __hip_atomic_store((GAS u64*)(ws + WS_KV0 + h * 128), token, RLX_AGENT);
        if (w == 0) wait_all();
      }
      DRAIN(); BAR();
      if (tid == 0) { __hip_atomic_store((GAS int*)(ws + FL_KVF + mb * 128 + h * 4), 0, RLX_AGENT); __hip_atomic_fetch_add((GAS int*)(ws + FL_ALL), 1, RLX_AGENT); }
    } else if (e < e2) {
      const rsrc_t ri = mkrs(p.w_in, 512 * 1792 * 2);
      const int i = z0 + e - e1, mb = i < 64 ? i >> 2 : (i - 64) / 3, ct = i < 64 ? 3 + (i & 3) : (i - 64) % 3;
      f32x4 acc[4][8];
      const int bvo = ct * 512 + b_voff(w, l, 3584);
      gemm_loop<2, true, true, 8, 128, 16384, 8192, 64 * 3584, 4 * 3584, false>([](int) { return false; }, smem, rxn, mb * 128 * 1024 + a_voff(w, l, 1024), ri, ri, bvo, bvo + 256, 128, acc, w, l);
      const int hb = 2 * ct + wn;
      u32x4 zo[4][4];
      float sm[4], sq[4];
#pragma unroll
      for (int i2 = 0; i2 < 4; ++i2) {
        sm[i2] = 0; sq[i2] = 0;
#pragma unroll
        for (int jp = 0; jp < 4; ++jp) {
          const f32x4 a = acc[i2][2 * jp], b = acc[i2][2 * jp + 1];
          zo[i2][jp] = (u32x4){pk_bf16(gelu_t(a[0]), gelu_t(a[1])), pk_bf16(gelu_t(a[2]), gelu_t(a[3])), pk_bf16(gelu_t(b[0]), gelu_t(b[1])), pk_bf16(gelu_t(b[2]), gelu_t(b[3]))};
        }
      }
      if (hb < 7) {
#pragma unroll
        for (int i2 = 0; i2 < 4; ++i2)
#pragma unroll
          for (int jp = 0; jp < 4; ++jp) st16s(rw, WS_Z + ((mb * 8 + 4 * wm + i2) * 28 + hb * 4 + jp) * 1024 + l * 16, zo[i2][jp]);
      } else {
#pragma unroll
        for (int i2 = 0; i2 < 4; ++i2) {
#pragma unroll
          for (int jp = 0; jp < 4; ++jp)
#pragma unroll
            for (int k = 0; k < 4; ++k) {
              const float a = blo(zo[i2][jp][k]), b = bhi(zo[i2][jp][k]);
              acc[i2][2 * jp + (k >> 1)][(2 * k) & 3] = a; acc[i2][2 * jp + (k >> 1)][(2 * k + 1) & 3] = b;
              sm[i2] += a + b; sq[i2] = __builtin_fmaf(a, a, __builtin_fmaf(b, b, sq[i2]));
            }
          sm[i2] = xsum32(xsum16(sm[i2])); sq[i2] = xsum32(xsum16(sq[i2]));
        }
        const float ps = lg == 0 ? sm[0] : lg == 1 ? sm[1] : lg == 2 ? sm[2] : sm[3], pq = lg == 0 ? sq[0] : lg == 1 ? sq[1] : lg == 2 ? sq[2] : sq[3];
        __builtin_amdgcn_raw_buffer_store_b64((u32x2){f2u(ps), f2u(pq)}, rw, WS_ST + ((mb * 7 + hb - 7) * 128 + 64 * wm + l) * 8, 0, 16);
      }
      if (ct >= 3) {
        signal_add(ws, FL_ZC + mb * 128);
        if (hb >= 7) {
          poll_ge(ws, FL_ZC + mb * 128, 3);
          __builtin_amdgcn_fence(__ATOMIC_ACQUIRE, "wavefront");
          asm volatile("" ::: "memory");
          u32x2 st[7];
#pragma unroll
          for (int k = 0; k < 7; ++k) st[k] = __builtin_amdgcn_raw_buffer_load_b64(rw, WS_ST + ((mb * 7 + k) * 128 + 64 * wm + l) * 8, 0, 16);
          float ts = 0, tq = 0;
#pragma unroll
          for (int k = 0; k < 7; ++k) { ts += u2f(st[k][0]); tq += u2f(st[k][1]); }
          const float mean = ts * (1.0f / 896.0f), var = fmaxf(tq * (1.0f / 896.0f) - mean * mean, 0.0f);
          const float rs = 1.0f / rbf(sqrtf(rbf(rbf(var) + 1e-5f))), ms = -rbf(mean) * rs;
#pragma unroll
          for (int i2 = 0; i2 < 4; ++i2) {
            const float r2 = __shfl(rs, 16 * i2 + lr), m2 = __shfl(ms, 16 * i2 + lr);
#pragma unroll
            for (int jp = 0; jp < 4; ++jp) {
              const f32x4 a = acc[i2][2 * jp], b = acc[i2][2 * jp + 1];
              const u32x4 o = {pk_bf16(__builtin_fmaf(a[0], r2, m2), __builtin_fmaf(a[1], r2, m2)), pk_bf16(__builtin_fmaf(a[2], r2, m2), __builtin_fmaf(a[3], r2, m2)),
                               pk_bf16(__builtin_fmaf(b[0], r2, m2), __builtin_fmaf(b[1], r2, m2)), pk_bf16(__builtin_fmaf(b[2], r2, m2), __builtin_fmaf(b[3], r2, m2))};
              st16s(rw, WS_Z2N + ((mb * 8 + 4 * wm + i2) * 28 + (hb - 7) * 4 + jp) * 1024 + l * 16, o);
            }
          }
        }
      }
      signal_add(ws, FL_ZN);
    } else if (e < e5) {
      const rsrc_t rsp = mkrs(p.w_sp, 8 * MB_), rz = mkrs(ws + WS_Z2N, 2048 * 1792);
      const int i = s0 + e - e4, mb = i / 7, ct = i % 7;
      f32x4 acc[4][4];
      u32x4 z1[4][2]; float bs[4];
      const int cz = (l & 15) ^ ((l >> 4) << 1);
      gemm_loop<1, true, true, 32, 128, 16 * 4096, 8 * 4096, 4 * 28672, 64, true>([&](int part) __attribute__((always_inline)) {
        if (part == 0) return false;
        u32x4 wa[19];
#pragma unroll
        for (int k = 0; k < 19; ++k) wa[k] = ld16(rsp, mb * 524288 + min(ct * 19 * 4096 + (k * 4 + w) * 1024, 523264) + l * 16);
        wait_acquire(ws, FL_ZN, 111);
#pragma unroll
        for (int k = 0; k < 19; ++k) asm volatile("" :: "v"(wa[k]));
#pragma unroll
        for (int i2 = 0; i2 < 4; ++i2) {
          bs[i2] = blo(p.b_sp[mb * 128 + 64 * wm + 16 * i2 + lr]);
#pragma unroll
          for (int jp = 0; jp < 2; ++jp) z1[i2][jp] = ld16(rw, WS_Z + ((mb * 8 + 4 * wm + i2) * 28 + ct * 4 + wn * 2 + jp) * 1024 + l * 16);
        }
        return false;
      }, smem, rsp, mb * 128 * 4096 + a_voff(w, l, 4096), rz, rz,
          w * 28672 + (ct * 4 + (cz >> 2)) * 1024 + (cz & 3) * 256 + (l >> 4) * 16, 0, 2048, acc, w, l);
#pragma unroll
      for (int i2 = 0; i2 < 4; ++i2)
#pragma unroll
        for (int jp = 0; jp < 2; ++jp) {
          const u32x4 z = z1[i2][jp];
          const f32x4 a = acc[i2][2 * jp], b = acc[i2][2 * jp + 1];
          const float c = bs[i2];
          const u32x4 o = {pk_bf16(blo(z[0]) * (a[0] + c), bhi(z[0]) * (a[1] + c)), pk_bf16(blo(z[1]) * (a[2] + c), bhi(z[1]) * (a[3] + c)),
                           pk_bf16(blo(z[2]) * (b[0] + c), bhi(z[2]) * (b[1] + c)), pk_bf16(blo(z[3]) * (b[2] + c), bhi(z[3]) * (b[3] + c))};
          st16s(rw, WS_U + ((mb * 8 + 4 * wm + i2) * 28 + ct * 4 + wn * 2 + jp) * 1024 + l * 16, o);
        }
      signal_add(ws, FL_UC + mb * 128);
    } else if (e >= e7) {
      const rsrc_t rq = mkrs(p.Wq, 4 * MB_);
      const int i = q0 + e - e7, hp = i >> 4, mb = i & 15;
      f32x4 acc[4][8];
      const int bvo = hp * 512 + b_voff(w, l, 8192);
      gemm_loop<2, false, true, 8, 128, 16384, 8192, 64 * 8192, 4 * 8192, false>([](int) { return false; }, smem, rxn, mb * 128 * 1024 + a_voff(w, l, 1024), rq, rq, bvo, bvo + 256, 128, acc, w, l);
      store_kstyle(rw, WS_QX + ((mb * 4 + 2 * wm) * 32 + 2 * hp + wn) * 8192, acc, l);
      signal_add(ws, FL_ALL);
    } else {
      const rsrc_t ru = mkrs(ws + WS_U, 2048 * 1792), ro = mkrs(p.w_out, 896 * 1024);
      const int i = n0 + e - e6, mb = i >> 2, ct = i & 3;
      f32x4 acc[4][4];
      u32x2 xv[4][4];
#pragma unroll
      for (int i2 = 0; i2 < 4; ++i2)
#pragma unroll
        for (int j = 0; j < 4; ++j) xv[i2][j] = *(const u32x2*)(p.x + (mb * 128 + 64 * wm + 16 * i2 + lr) * 512 + ct * 128 + wn * 64 + j * 16 + lg * 4);
      const int cu = (l & 7) ^ (l >> 3);
      gemm_loop<1, false, true, 14, 2048, 28672, 128, 64 * 1024, 4 * 1024, false>(
          [&](int part) __attribute__((always_inline)) { if (part == 1) wait_acquire(ws, FL_UC + mb * 128, 6); return false; }, smem, ru, (mb * 8 + 2 * w) * 28672 + (cu >> 2) * 1024 + (cu & 3) * 256 + (l >> 3) * 16,
          ro, ro, ct * 256 + b_voff(w, l, 1024), 0, 128, acc, w, l);
#pragma unroll
      for (int i2 = 0; i2 < 4; ++i2)
#pragma unroll
        for (int j = 0; j < 4; ++j) {
          const f32x4 a = acc[i2][j];
          const u32x2 x2 = xv[i2][j];
          const u32x4 o = {f2u(a[0] + blo(x2[0])), f2u(a[1] + bhi(x2[0])), f2u(a[2] + blo(x2[1])), f2u(a[3] + bhi(x2[1]))};
          st16s(rw, WS_BASE + ((mb * 8 + 4 * wm + i2) * 32 + ct * 8 + wn * 4 + j) * 1024 + l * 16, o);
        }
#pragma unroll
      for (int k = 0; k < 8; ++k) st16s(rw, WS_XS + SL * 0x80000 + (i * 8 + k) * 4096 + tid * 16, (u32x4){~0u, ~0u, ~0u, ~0u});
      signal_add(ws, FL_ALL);
    }
    if (tid == 0) *(volatile LAS int*)(smem) = __hip_atomic_fetch_add((GAS int*)(ws + FL_HEAD + xg * 128), 1, RLX_AGENT) + 33;
    BAR();
    e = __builtin_amdgcn_readfirstlane(*(volatile LAS int*)(smem));
  }
  LANE_VARS
  const int kq = idx >> 2, cs = idx & 3, h = 4 * kq + w;
  bf8 WO[2][16];
  {
    const rsrc_t rwo = mkrs(p.Wo, 4 * MB_);
    BAR();
    const int vo = (kq * 512 + 128 * w + (l >> 4)) * 1024 + cs * 256 + (l & 15) * 16;
#pragma unroll
    for (int i = 0; i < 32; ++i) dma16(rwo, w * 32768, i * 1024, vo, i * 4096);
    DRAIN(); BAR();
    const int q4 = (l >> 2) & 3, p4 = l & 3;
#pragma unroll
    for (int t = 0; t < 2; ++t)
#pragma unroll
      for (int ks = 0; ks < 16; ++ks) {
        const int a = (32 * ks + 4 * lg + q4) * 256 + (32 * w + 16 * t + 4 * p4) * 2;
        const bf4 b0 = trread(smem, a), b1 = trread(smem, a + 16 * 256);
        WO[t][ks] = __builtin_shufflevector(b0, b1, 0, 1, 2, 3, 4, 5, 6, 7);
      }
  }
  wait_acquire(ws, FL_ALL, 831);
  const float cs2 = 0.08838834764831845f * 1.4426950408889634f;
  const u32x4 ones8 = {0x3F803F80u, 0x3F803F80u, 0x3F803F80u, 0x3F803F80u};
  const unsigned oh = lg >= 2 ? 0x3F803F80u : 0u;
  const f32x4 z4 = {0, 0, 0, 0};
  u32x4 Q[2][4], KX[2][4], VX[8], KM[4], VM[4];
  int seen = 0;
  auto fetch = [&](int s) __attribute__((always_inline)) {
    const int fo = (s * 32 + h) * 8192 + l * 16;
#pragma unroll
    for (int i = 0; i < 8; ++i) { Q[i >> 2][i & 3] = ld16(rw, WS_QX + fo + i * 1024); KX[i >> 2][i & 3] = ld16(rw, WS_KX + fo + i * 1024); }
#pragma unroll
    for (int i = 0; i < 8; ++i) VX[i] = ld16(rw, WS_VX + fo + i * 1024);
  };
  auto reduce = [&](int s) __attribute__((always_inline)) {
    if (w < 2) {
      const int slab = WS_XS + (s * 4 + cs) * 8 * 16384;
      const int row = s * 32 + 16 * w + lr, col = cs * 128 + 16 * kq + 4 * lg;
      const u32x4 bs = ld16(rw, WS_BASE + ((s * 2 + w) * 32 + cs * 8 + kq) * 1024 + l * 16);
      _Pragma("unroll 1") for (int sp = 0; sp < SPIN_MAX && s < SL; ++sp) {
        if (__builtin_amdgcn_readfirstlane(__hip_atomic_load((GAS int*)(ws + FL_XC + (s * 4 + cs) * 128), RLX_AGENT)) >= 7) break;
        __builtin_amdgcn_s_sleep(1);
      }
      __builtin_amdgcn_fence(__ATOMIC_ACQUIRE, "wavefront");
      u32x4 v[8];
      if (s >= SL) __builtin_amdgcn_s_sleep(RSLEEP);
      _Pragma("unroll 1") for (int sp = 0; sp < SPIN_MAX; ++sp) {
        asm volatile("" ::: "memory");
        unsigned m = 0;
#pragma unroll
        for (int k = 0; k < 8; ++k) v[k] = ld16s(rw, slab + k * 16384 + (kq * 2 + w) * 1024 + l * 16);
#pragma unroll
        for (int k = 0; k < 8; ++k) m = max(max(m, max(v[k][0], v[k][1])), max(v[k][2], v[k][3]));
        if (s < SL || __all(m != ~0u)) break;
      }
      float a[4];
#pragma unroll
      for (int r = 0; r < 4; ++r)
        a[r] = u2f(bs[r]) + (((u2f(v[0][r]) + u2f(v[1][r])) + (u2f(v[2][r]) + u2f(v[3][r]))) + ((u2f(v[4][r]) + u2f(v[5][r])) + (u2f(v[6][r]) + u2f(v[7][r]))));
      *(u32x2*)(p.out + row * 512 + col) = (u32x2){pk_bf16(a[0], a[1]), pk_bf16(a[2], a[3])};
    }
  };
  fetch(xg);
#pragma unroll 1
  for (int s = xg; s < 64; s += 6) {
    f32x4 sx00 = z4, sx01 = z4, sx11 = z4;
#pragma unroll
    for (int u = 0; u < 4; ++u) { sx00 = mf32u(KX[0][u], Q[0][u], sx00); sx01 = mf32u(KX[0][u], Q[1][u], sx01); sx11 = mf32u(KX[1][u], Q[1][u], sx11); }
#pragma unroll
    for (int r = 0; r < 4; ++r) if (4 * lg + r > lr) { sx00[r] = -INFINITY; sx11[r] = -INFINITY; }
    float mx0 = fmaxf(fmaxf(sx00[0], sx00[1]), fmaxf(sx00[2], sx00[3]));
    float mx1 = fmaxf(fmaxf(fmaxf(sx01[0], sx01[1]), fmaxf(sx01[2], sx01[3])), fmaxf(fmaxf(sx11[0], sx11[1]), fmaxf(sx11[2], sx11[3])));
    asm volatile("" : "+v"(mx0), "+v"(mx1));
    if (seen <= s) {
      if (s >= 6) reduce(s - 6);
      const int need = s >= SL ? s - 1 : s + 1;
      _Pragma("unroll 1") for (int sp = 0; sp < SPIN_MAX; ++sp) {
        const int f = __hip_atomic_load((GAS int*)(ws + FL_PF + (h + (l & 32)) * 128), RLX_AGENT);
        seen = min(__builtin_amdgcn_readlane(f, 0), __builtin_amdgcn_readlane(f, 32));
        if (seen >= need) break;
        __builtin_amdgcn_s_sleep(2);
      }
      __builtin_amdgcn_fence(__ATOMIC_ACQUIRE, "wavefront");
      const int ko = WS_KM + (s * 32 + h) * 4096, vo = WS_VM + (s * 32 + h) * 4096;
      _Pragma("unroll 1") for (int sp = 0; sp < SPIN_MAX; ++sp) {
        asm volatile("" ::: "memory");
        unsigned m = 0;
#pragma unroll
        for (int i = 0; i < 4; ++i) { KM[i] = ld16s(rw, ko + i * 1024 + l * 16); VM[i] = ld16s(rw, vo + i * 1024 + l * 16); }
#pragma unroll
        for (int i = 0; i < 4; ++i) m = max(max(m, max(max(KM[i][0], KM[i][1]), max(KM[i][2], KM[i][3]))), max(max(VM[i][0], VM[i][1]), max(VM[i][2], VM[i][3])));
        if (s < SL || __all(m != ~0u)) break;
      }
    } else {
#pragma unroll
      for (int i = 0; i < 4; ++i) { KM[i] = ld16s(rw, WS_KM + (s * 32 + h) * 4096 + i * 1024 + l * 16); VM[i] = ld16s(rw, WS_VM + (s * 32 + h) * 4096 + i * 1024 + l * 16); }
      if (s >= 6) reduce(s - 6);
    }
    char* xb = smem + w * 8192 + l * 16;
    {
      f32x4 sm0 = z4, sm1 = z4;
#pragma unroll
      for (int u = 0; u < 4; ++u) { sm0 = mf32u(KM[u], Q[0][u], sm0); sm1 = mf32u(KM[u], Q[1][u], sm1); }
      float m0 = fmaxf(mx0, fmaxf(fmaxf(sm0[0], sm0[1]), fmaxf(sm0[2], sm0[3]))), m1 = fmaxf(mx1, fmaxf(fmaxf(sm1[0], sm1[1]), fmaxf(sm1[2], sm1[3])));
      m0 = xmax16(m0); m1 = xmax16(m1); m0 = xmax32(m0); m1 = xmax32(m1);
#define EX(v, m) __builtin_amdgcn_exp2f(((v) - (m)) * cs2)
      const u32x4 px[2] = {{pk_bf16(EX(sx00[0], m0), EX(sx00[1], m0)), pk_bf16(EX(sx00[2], m0), EX(sx00[3], m0)), 0u, 0u},
                           {pk_bf16(EX(sx01[0], m1), EX(sx01[1], m1)), pk_bf16(EX(sx01[2], m1), EX(sx01[3], m1)),
                            pk_bf16(EX(sx11[0], m1), EX(sx11[1], m1)), pk_bf16(EX(sx11[2], m1), EX(sx11[3], m1))}};
      const u32x4 pm[2] = {{pk_bf16(EX(sm0[0], m0), EX(sm0[1], m0)), pk_bf16(EX(sm0[2], m0), EX(sm0[3], m0)), 0u, 0u},
                           {pk_bf16(EX(sm1[0], m1), EX(sm1[1], m1)), pk_bf16(EX(sm1[2], m1), EX(sm1[3], m1)), 0u, 0u}};
#undef EX
      f32x4 ls[2], o[2][8];
#pragma unroll
      for (int qt = 0; qt < 2; ++qt) ls[qt] = mf32u((u32x4){oh, oh, 0u, 0u}, pm[qt], mf32u(ones8, px[qt], z4));
#pragma unroll
      for (int T = 0; T < 8; ++T)
#pragma unroll
        for (int qt = 0; qt < 2; ++qt)
          o[qt][T] = mf32u((u32x4){VM[T >> 1][2 * (T & 1)], VM[T >> 1][2 * (T & 1) + 1], 0u, 0u}, pm[qt], mf32u(VX[T], px[qt], z4));
#pragma unroll
      for (int u = 0; u < 4; ++u)
#pragma unroll
        for (int qt = 0; qt < 2; ++qt) {
          const float rl = __builtin_amdgcn_rcpf(ls[qt][0]);
          const f32x4 o0 = o[qt][2 * u], o1 = o[qt][2 * u + 1];
          *(u32x4*)(xb + (u * 2 + qt) * 1024) =
              (u32x4){pk_bf16(o0[0] * rl, o0[1] * rl), pk_bf16(o0[2] * rl, o0[3] * rl), pk_bf16(o1[0] * rl, o1[1] * rl), pk_bf16(o1[2] * rl, o1[3] * rl)};
        }
    }
    BAR();
    f32x4 c[2][2] = {{z4, z4}, {z4, z4}};
    {
      bf8 ob[32];
#pragma unroll
      for (int i = 0; i < 12; ++i) ob[i] = *(const bf8*)(smem + i * 1024 + l * 16);
      __builtin_amdgcn_sched_barrier(0);
#pragma unroll
      for (int i = 0; i < 32; ++i) {
        if (i + 12 < 32) ob[i + 12] = *(const bf8*)(smem + (i + 12) * 1024 + l * 16);
        c[0][i & 1] = mf32(WO[0][i >> 1], ob[i], c[0][i & 1]);
        c[1][i & 1] = mf32(WO[1][i >> 1], ob[i], c[1][i & 1]);
        __builtin_amdgcn_sched_barrier(0);
      }
    }
    const int slab = WS_XS + (s * 4 + cs) * 8 * 16384;
#pragma unroll
    for (int t = 0; t < 2; ++t)
#pragma unroll
      for (int qt = 0; qt < 2; ++qt) {
        const u32x4 o = __builtin_bit_cast(u32x4, c[t][qt]);
        st16s(rw, slab + kq * 16384 + ((w * 2 + t) * 2 + qt) * 1024 + l * 16, (u32x4){min(o[0], ~1u), min(o[1], ~1u), min(o[2], ~1u), min(o[3], ~1u)});
      }
    if (s >= SL) { reduce(s); break; }
    __builtin_amdgcn_sched_barrier(0);
    fetch(s + 6 < 64 ? s + 6 : s);
    __builtin_amdgcn_sched_barrier(0);
    asm volatile("s_waitcnt vmcnt(24)" ::: "memory");
    BAR();
    if (tid == 0) __hip_atomic_fetch_add((GAS int*)(ws + FL_XC + (s * 4 + cs) * 128), 1, RLX_AGENT);
    if (s + 6 >= 64) reduce(s);
  }
  if (xg == 3 && idx == 0 && tid == 0) __hip_atomic_store((GAS u64*)(ws + WS_TOK), tok(), RLX_AGENT);
}

__device__ __forceinline__ void chain(const Args& p) {
  constexpr int LDS_QK = 8192, LDS_O = 12288, LDS_WIN = 16384, LDS_WO = 65536;
  const int h = blockIdx.x >> 3, r = blockIdx.x & 1, c = r * 32 + h;
  const int tid = threadIdx.x, l = tid & 63, w = __builtin_amdgcn_readfirstlane(tid >> 6);
  const int lr = l & 15, lg = l >> 4, q4 = (l >> 2) & 3, p4 = l & 3;
  constexpr int sel = 0;
  const int trrow = 16 * (lg >> 1) + 4 * (lg & 1) + (q4 & 1) + 8 * (q4 >> 1);
  const int tile4 = ((lg & 1) << 1) | (lg >> 1);
  u32x16 va; u32x2 vb; u32x16 sa; u32x8 sb;
  va[0] = l * 16;
  va[1] = (l & 8) ? 0xEEEE : 0x4444;
  va[2] = remap(WS_PB + (r * 32 + 8 * w + 2 * tile4 + (lr >> 3)) * 4096 + h * 128 + (lr & 7) * 16, sel);
  va[3] = LDS_QK + (4 * lg + q4) * 32 + p4 * 8;
  va[4] = LDS_QK + (4 * lg + q4) * 32 + 16 + (p4 & 1) * 8;
  va[5] = LDS_QK + (32 * w + lr) * 32 + 8 * lg;
  va[6] = LDS_O + (w >> 1) * 1024 + (32 * (w & 1) + 8 * (lg >> 1) + lr) * 16 + 8 * (lg & 1);
  va[7] = w * 2048 + l * 16;
  va[8] = ((w + 1) & 3) * 1024 + l * 16;
  va[9] = ((w + 2) & 3) * 1024 + l * 16;
  va[10] = ((w + 3) & 3) * 1024 + l * 16;
  va[11] = LDS_WO + w * 16384 + l * 16;
  va[12] = (16 * w + lg) * 8192 + h * 256 + lr * 16;
  va[13] = LDS_WIN + (16 * w + lg) * 272 + lr * 16;
  va[14] = LDS_WIN + trrow * 272 + (32 * w + 4 * p4) * 2;
  va[15] = h * 131072 + (l >> 3) * 1024 + (256 * r + 64 * w + (l & 7) * 8) * 2;
  vb[0] = LDS_WO + w * 16384 + trrow * 128 + 8 * p4;
  vb[1] = ((l & 7) * 512 + 256 * r + 64 * w + 16 * lg + 8 * ((l >> 3) & 1)) * 2;
  const unsigned long long ptrs[6] = {(unsigned long long)p.ws, (unsigned long long)p.Wq, (unsigned long long)p.Wk, (unsigned long long)p.Wv,
                                      (unsigned long long)p.Wo, (unsigned long long)p.mem0};
#pragma unroll
  for (int i = 0; i < 6; ++i) { sa[2 * i] = (unsigned)ptrs[i]; sa[2 * i + 1] = (unsigned)(ptrs[i] >> 32); }
  sa[12] = w; sa[13] = r; sa[14] = p.da & 0xFFFF; sa[15] = p.db;
  sb[0] = remap(WS_MA, sel) + (4 * r + w) * 1024; sb[1] = remap(WS_MA + 8192 + c * 128, sel); sb[2] = remap(WS_PB + c * 4096, sel);
  sb[3] = WS_KX + h * 8192; sb[4] = WS_VX + h * 8192; sb[5] = (r == 0 ? WS_KM : WS_VM) + h * 4096 + w * 1024;
  sb[6] = FL_KVF + h * 4; sb[7] = FL_PF + c * 128;
  asm volatile(
"s_mov_b32 s52, s36\n"
"s_mov_b32 s53, s37\n"
"s_mov_b32 s54, 0x8000000\n"
"s_mov_b32 s55, 0x20000\n"
"s_mov_b32 s56, s38\n"
"s_mov_b32 s57, s39\n"
"s_mov_b32 s58, 0x400000\n"
"s_mov_b32 s59, 0x20000\n"
"s_mov_b32 s60, s40\n"
"s_mov_b32 s61, s41\n"
"s_mov_b32 s62, 0x400000\n"
"s_mov_b32 s63, 0x20000\n"
"s_mov_b32 s64, s42\n"
"s_mov_b32 s65, s43\n"
"s_mov_b32 s66, 0x400000\n"
"s_mov_b32 s67, 0x20000\n"
"s_mov_b32 s68, s44\n"
"s_mov_b32 s69, s45\n"
"s_mov_b32 s70, 0x400000\n"
"s_mov_b32 s71, 0x20000\n"
"s_mov_b32 s79, 0x1b0000\n"
"s_load_dwordx2 s[26:27], s[36:37], s79\n"
"s_memtime s[22:23]\n"
"s_memrealtime s[24:25]\n"
"global_load_dwordx4 v[96:99], v17, s[46:47]\n"
"v_xor_b32 v19, 0x200, v17\n"
"global_load_dwordx4 v[100:103], v19, s[46:47]\n"
"s_mov_b32 s79, 0\n"
"buffer_load_dwordx4 v[32:35], v15, s[68:71], s79 offen\n"
"s_mov_b32 s79, 8192\n"
"buffer_load_dwordx4 v[36:39], v15, s[68:71], s79 offen\n"
"s_mov_b32 s79, 16384\n"
"buffer_load_dwordx4 v[40:43], v15, s[68:71], s79 offen\n"
"s_mov_b32 s79, 24576\n"
"buffer_load_dwordx4 v[44:47], v15, s[68:71], s79 offen\n"
"s_mov_b32 s79, 32768\n"
"buffer_load_dwordx4 v[48:51], v15, s[68:71], s79 offen\n"
"s_mov_b32 s79, 40960\n"
"buffer_load_dwordx4 v[52:55], v15, s[68:71], s79 offen\n"
"s_mov_b32 s79, 49152\n"
"buffer_load_dwordx4 v[56:59], v15, s[68:71], s79 offen\n"
"s_mov_b32 s79, 57344\n"
"buffer_load_dwordx4 v[60:63], v15, s[68:71], s79 offen\n"
"s_mov_b32 s79, 65536\n"
"buffer_load_dwordx4 v[64:67], v15, s[68:71], s79 offen\n"
"s_mov_b32 s79, 73728\n"
"buffer_load_dwordx4 v[68:71], v15, s[68:71], s79 offen\n"
"s_mov_b32 s79, 81920\n"
"buffer_load_dwordx4 v[72:75], v15, s[68:71], s79 offen\n"
"s_mov_b32 s79, 90112\n"
"buffer_load_dwordx4 v[76:79], v15, s[68:71], s79 offen\n"
"s_mov_b32 s79, 98304\n"
"buffer_load_dwordx4 v[80:83], v15, s[68:71], s79 offen\n"
"s_mov_b32 s79, 106496\n"
"buffer_load_dwordx4 v[84:87], v15, s[68:71], s79 offen\n"
"s_mov_b32 s79, 114688\n"
"buffer_load_dwordx4 v[88:91], v15, s[68:71], s79 offen\n"
"s_mov_b32 s79, 122880\n"
"buffer_load_dwordx4 v[92:95], v15, s[68:71], s79 offen\n"
"v_lshrrev_b32 v24, 4, v0\n"
"v_and_b32 v25, 15, v24\n"
"v_cmp_gt_u32 s[86:87], 8, v25\n"
"v_cmp_lt_u32 s[88:89], 31, v24\n"
"v_lshrrev_b32 v25, 1, v25\n"
"v_cmp_eq_u32 s[90:91], 0, v25\n"
"v_cmp_eq_u32 s[92:93], 1, v25\n"
"v_cmp_eq_u32 s[94:95], 2, v25\n"
"v_cmp_eq_u32 s[96:97], 3, v25\n"
"s_mov_b32 s76, 0x3e0293ee\n"
"s_mov_b32 s77, 0x07060302\n"
"s_mov_b32 s78, 0x8000\n"
"s_cmp_eq_u32 s48, 1\n"
"s_cselect_b32 s82, 1, 0\n"
"s_mov_b32 s83, 0\n"
"s_lshl_b32 s84, s49, 21\n"
"s_cmp_eq_u32 s48, 1\n"
"s_cbranch_scc1 L_su1_%=\n"
"s_cmp_eq_u32 s48, 2\n"
"s_cbranch_scc1 L_su2_%=\n"
"s_cmp_eq_u32 s48, 3\n"
"s_cbranch_scc1 L_su3_%=\n"
"L_su0_%=:\n"
"s_xor_b32 s79, s84, 0x0\n"
"buffer_load_dwordx4 a[0:3], v12, s[56:59], s79 offen\n"
"s_xor_b32 s79, s84, 0x8000\n"
"buffer_load_dwordx4 a[4:7], v12, s[56:59], s79 offen\n"
"s_xor_b32 s79, s84, 0x10000\n"
"buffer_load_dwordx4 a[64:67], v12, s[56:59], s79 offen\n"
"s_xor_b32 s79, s84, 0x18000\n"
"buffer_load_dwordx4 a[68:71], v12, s[56:59], s79 offen\n"
"s_xor_b32 s79, s84, 0x80000\n"
"buffer_load_dwordx4 a[8:11], v12, s[56:59], s79 offen\n"
"s_xor_b32 s79, s84, 0x88000\n"
"buffer_load_dwordx4 a[12:15], v12, s[56:59], s79 offen\n"
"s_xor_b32 s79, s84, 0x90000\n"
"buffer_load_dwordx4 a[72:75], v12, s[56:59], s79 offen\n"
"s_xor_b32 s79, s84, 0x98000\n"
"buffer_load_dwordx4 a[76:79], v12, s[56:59], s79 offen\n"
"s_xor_b32 s79, s84, 0x100000\n"
"buffer_load_dwordx4 a[16:19], v12, s[56:59], s79 offen\n"
"s_xor_b32 s79, s84, 0x108000\n"
"buffer_load_dwordx4 a[20:23], v12, s[56:59], s79 offen\n"
"s_xor_b32 s79, s84, 0x110000\n"
"buffer_load_dwordx4 a[80:83], v12, s[56:59], s79 offen\n"
"s_xor_b32 s79, s84, 0x118000\n"
"buffer_load_dwordx4 a[84:87], v12, s[56:59], s79 offen\n"
"s_xor_b32 s79, s84, 0x180000\n"
"buffer_load_dwordx4 a[24:27], v12, s[56:59], s79 offen\n"
"s_xor_b32 s79, s84, 0x188000\n"
"buffer_load_dwordx4 a[28:31], v12, s[56:59], s79 offen\n"
"s_xor_b32 s79, s84, 0x190000\n"
"buffer_load_dwordx4 a[88:91], v12, s[56:59], s79 offen\n"
"s_xor_b32 s79, s84, 0x198000\n"
"buffer_load_dwordx4 a[92:95], v12, s[56:59], s79 offen\n"
"s_xor_b32 s79, s84, 0x200000\n"
"buffer_load_dwordx4 a[32:35], v12, s[56:59], s79 offen\n"
"s_xor_b32 s79, s84, 0x208000\n"
"buffer_load_dwordx4 a[36:39], v12, s[56:59], s79 offen\n"
"s_xor_b32 s79, s84, 0x210000\n"
"buffer_load_dwordx4 a[96:99], v12, s[56:59], s79 offen\n"
"s_xor_b32 s79, s84, 0x218000\n"
"buffer_load_dwordx4 a[100:103], v12, s[56:59], s79 offen\n"
"s_xor_b32 s79, s84, 0x280000\n"
"buffer_load_dwordx4 a[40:43], v12, s[56:59], s79 offen\n"
"s_xor_b32 s79, s84, 0x288000\n"
"buffer_load_dwordx4 a[44:47], v12, s[56:59], s79 offen\n"
"s_xor_b32 s79, s84, 0x290000\n"
"buffer_load_dwordx4 a[104:107], v12, s[56:59], s79 offen\n"
"s_xor_b32 s79, s84, 0x298000\n"
"buffer_load_dwordx4 a[108:111], v12, s[56:59], s79 offen\n"
"s_xor_b32 s79, s84, 0x300000\n"
"buffer_load_dwordx4 a[48:51], v12, s[56:59], s79 offen\n"
"s_xor_b32 s79, s84, 0x308000\n"
"buffer_load_dwordx4 a[52:55], v12, s[56:59], s79 offen\n"
"s_xor_b32 s79, s84, 0x310000\n"
"buffer_load_dwordx4 a[112:115], v12, s[56:59], s79 offen\n"
"s_xor_b32 s79, s84, 0x318000\n"
"buffer_load_dwordx4 a[116:119], v12, s[56:59], s79 offen\n"
"s_xor_b32 s79, s84, 0x380000\n"
"buffer_load_dwordx4 a[56:59], v12, s[56:59], s79 offen\n"
"s_xor_b32 s79, s84, 0x388000\n"
"buffer_load_dwordx4 a[60:63], v12, s[56:59], s79 offen\n"
"s_xor_b32 s79, s84, 0x390000\n"
"buffer_load_dwordx4 a[120:123], v12, s[56:59], s79 offen\n"
"s_xor_b32 s79, s84, 0x398000\n"
"buffer_load_dwordx4 a[124:127], v12, s[56:59], s79 offen\n"
"s_xor_b32 s79, s84, 0x0\n"
"buffer_load_dwordx4 a[128:131], v12, s[60:63], s79 offen\n"
"s_xor_b32 s79, s84, 0x8000\n"
"buffer_load_dwordx4 a[132:135], v12, s[60:63], s79 offen\n"
"s_xor_b32 s79, s84, 0x10000\n"
"buffer_load_dwordx4 a[192:195], v12, s[60:63], s79 offen\n"
"s_xor_b32 s79, s84, 0x18000\n"
"buffer_load_dwordx4 a[196:199], v12, s[60:63], s79 offen\n"
"s_xor_b32 s79, s84, 0x80000\n"
"buffer_load_dwordx4 a[136:139], v12, s[60:63], s79 offen\n"
"s_xor_b32 s79, s84, 0x88000\n"
"buffer_load_dwordx4 a[140:143], v12, s[60:63], s79 offen\n"
"s_xor_b32 s79, s84, 0x90000\n"
"buffer_load_dwordx4 a[200:203], v12, s[60:63], s79 offen\n"
"s_xor_b32 s79, s84, 0x98000\n"
"buffer_load_dwordx4 a[204:207], v12, s[60:63], s79 offen\n"
"s_waitcnt vmcnt(40)\n"
"ds_write_b128 v11, v[32:35] offset:0\n"
"ds_write_b128 v11, v[36:39] offset:1024\n"
"ds_write_b128 v11, v[40:43] offset:2048\n"
"ds_write_b128 v11, v[44:47] offset:3072\n"
"ds_write_b128 v11, v[48:51] offset:4096\n"
"ds_write_b128 v11, v[52:55] offset:5120\n"
"ds_write_b128 v11, v[56:59] offset:6144\n"
"ds_write_b128 v11, v[60:63] offset:7168\n"
"ds_write_b128 v11, v[64:67] offset:8192\n"
"ds_write_b128 v11, v[68:71] offset:9216\n"
"ds_write_b128 v11, v[72:75] offset:10240\n"
"ds_write_b128 v11, v[76:79] offset:11264\n"
"ds_write_b128 v11, v[80:83] offset:12288\n"
"ds_write_b128 v11, v[84:87] offset:13312\n"
"ds_write_b128 v11, v[88:91] offset:14336\n"
"ds_write_b128 v11, v[92:95] offset:15360\n"
"ds_read_b64_tr_b16 v[128:129], v16 offset:0\n"
"ds_read_b64_tr_b16 v[130:131], v16 offset:256\n"
"ds_read_b64_tr_b16 v[132:133], v16 offset:4096\n"
"ds_read_b64_tr_b16 v[134:135], v16 offset:4352\n"
"ds_read_b64_tr_b16 v[136:137], v16 offset:32\n"
"ds_read_b64_tr_b16 v[138:139], v16 offset:288\n"
"ds_read_b64_tr_b16 v[140:141], v16 offset:4128\n"
"ds_read_b64_tr_b16 v[142:143], v16 offset:4384\n"
"ds_read_b64_tr_b16 v[32:33], v16 offset:64\n"
"ds_read_b64_tr_b16 v[34:35], v16 offset:320\n"
"ds_read_b64_tr_b16 v[36:37], v16 offset:4160\n"
"ds_read_b64_tr_b16 v[38:39], v16 offset:4416\n"
"ds_read_b64_tr_b16 v[40:41], v16 offset:96\n"
"ds_read_b64_tr_b16 v[42:43], v16 offset:352\n"
"ds_read_b64_tr_b16 v[44:45], v16 offset:4192\n"
"ds_read_b64_tr_b16 v[46:47], v16 offset:4448\n"
"ds_read_b64_tr_b16 v[192:193], v16 offset:8192\n"
"ds_read_b64_tr_b16 v[194:195], v16 offset:8448\n"
"ds_read_b64_tr_b16 v[196:197], v16 offset:12288\n"
"ds_read_b64_tr_b16 v[198:199], v16 offset:12544\n"
"ds_read_b64_tr_b16 v[200:201], v16 offset:8224\n"
"ds_read_b64_tr_b16 v[202:203], v16 offset:8480\n"
"ds_read_b64_tr_b16 v[204:205], v16 offset:12320\n"
"ds_read_b64_tr_b16 v[206:207], v16 offset:12576\n"
"ds_read_b64_tr_b16 v[48:49], v16 offset:8256\n"
"ds_read_b64_tr_b16 v[50:51], v16 offset:8512\n"
"ds_read_b64_tr_b16 v[52:53], v16 offset:12352\n"
"ds_read_b64_tr_b16 v[54:55], v16 offset:12608\n"
"ds_read_b64_tr_b16 v[56:57], v16 offset:8288\n"
"ds_read_b64_tr_b16 v[58:59], v16 offset:8544\n"
"ds_read_b64_tr_b16 v[60:61], v16 offset:12384\n"
"ds_read_b64_tr_b16 v[62:63], v16 offset:12640\n"
"s_waitcnt lgkmcnt(0)\n"
"ds_write_b128 v11, v[32:35] offset:0\n"
"ds_write_b128 v11, v[36:39] offset:1024\n"
"ds_write_b128 v11, v[40:43] offset:2048\n"
"ds_write_b128 v11, v[44:47] offset:3072\n"
"ds_write_b128 v11, v[48:51] offset:4096\n"
"ds_write_b128 v11, v[52:55] offset:5120\n"
"ds_write_b128 v11, v[56:59] offset:6144\n"
"ds_write_b128 v11, v[60:63] offset:7168\n"
"s_xor_b32 s79, s84, 0x100000\n"
"buffer_load_dwordx4 a[144:147], v12, s[60:63], s79 offen\n"
"s_xor_b32 s79, s84, 0x108000\n"
"buffer_load_dwordx4 a[148:151], v12, s[60:63], s79 offen\n"
"s_xor_b32 s79, s84, 0x110000\n"
"buffer_load_dwordx4 a[208:211], v12, s[60:63], s79 offen\n"
"s_xor_b32 s79, s84, 0x118000\n"
"buffer_load_dwordx4 a[212:215], v12, s[60:63], s79 offen\n"
"s_waitcnt vmcnt(40)\n"
"ds_write_b128 v13, a[0:3] offset:0\n"
"ds_write_b128 v13, a[4:7] offset:1088\n"
"ds_write_b128 v13, a[64:67] offset:2176\n"
"ds_write_b128 v13, a[68:71] offset:3264\n"
"s_waitcnt lgkmcnt(0)\n"
"s_barrier\n"
"ds_read_b64_tr_b16 a[0:1], v14 offset:0\n"
"ds_read_b64_tr_b16 a[2:3], v14 offset:544\n"
"ds_read_b64_tr_b16 a[4:5], v14 offset:8704\n"
"ds_read_b64_tr_b16 a[6:7], v14 offset:9248\n"
"ds_read_b64_tr_b16 a[64:65], v14 offset:32\n"
"ds_read_b64_tr_b16 a[66:67], v14 offset:576\n"
"ds_read_b64_tr_b16 a[68:69], v14 offset:8736\n"
"ds_read_b64_tr_b16 a[70:71], v14 offset:9280\n"
"s_xor_b32 s79, s84, 0x180000\n"
"buffer_load_dwordx4 a[152:155], v12, s[60:63], s79 offen\n"
"s_xor_b32 s79, s84, 0x188000\n"
"buffer_load_dwordx4 a[156:159], v12, s[60:63], s79 offen\n"
"s_xor_b32 s79, s84, 0x190000\n"
"buffer_load_dwordx4 a[216:219], v12, s[60:63], s79 offen\n"
"s_xor_b32 s79, s84, 0x198000\n"
"buffer_load_dwordx4 a[220:223], v12, s[60:63], s79 offen\n"
"s_waitcnt vmcnt(40)\n"
"ds_write_b128 v13, a[8:11] offset:17408\n"
"ds_write_b128 v13, a[12:15] offset:18496\n"
"ds_write_b128 v13, a[72:75] offset:19584\n"
"ds_write_b128 v13, a[76:79] offset:20672\n"
"s_waitcnt lgkmcnt(0)\n"
"s_barrier\n"
"ds_read_b64_tr_b16 a[8:9], v14 offset:17408\n"
"ds_read_b64_tr_b16 a[10:11], v14 offset:17952\n"
"ds_read_b64_tr_b16 a[12:13], v14 offset:26112\n"
"ds_read_b64_tr_b16 a[14:15], v14 offset:26656\n"
"ds_read_b64_tr_b16 a[72:73], v14 offset:17440\n"
"ds_read_b64_tr_b16 a[74:75], v14 offset:17984\n"
"ds_read_b64_tr_b16 a[76:77], v14 offset:26144\n"
"ds_read_b64_tr_b16 a[78:79], v14 offset:26688\n"
"s_xor_b32 s79, s84, 0x200000\n"
"buffer_load_dwordx4 a[160:163], v12, s[60:63], s79 offen\n"
"s_xor_b32 s79, s84, 0x208000\n"
"buffer_load_dwordx4 a[164:167], v12, s[60:63], s79 offen\n"
"s_xor_b32 s79, s84, 0x210000\n"
"buffer_load_dwordx4 a[224:227], v12, s[60:63], s79 offen\n"
"s_xor_b32 s79, s84, 0x218000\n"
"buffer_load_dwordx4 a[228:231], v12, s[60:63], s79 offen\n"
"s_waitcnt vmcnt(40)\n"
"ds_write_b128 v13, a[16:19] offset:0\n"
"ds_write_b128 v13, a[20:23] offset:1088\n"
"ds_write_b128 v13, a[80:83] offset:2176\n"
"ds_write_b128 v13, a[84:87] offset:3264\n"
"s_waitcnt lgkmcnt(0)\n"
"s_barrier\n"
"ds_read_b64_tr_b16 a[16:17], v14 offset:0\n"
"ds_read_b64_tr_b16 a[18:19], v14 offset:544\n"
"ds_read_b64_tr_b16 a[20:21], v14 offset:8704\n"
"ds_read_b64_tr_b16 a[22:23], v14 offset:9248\n"
"ds_read_b64_tr_b16 a[80:81], v14 offset:32\n"
"ds_read_b64_tr_b16 a[82:83], v14 offset:576\n"
"ds_read_b64_tr_b16 a[84:85], v14 offset:8736\n"
"ds_read_b64_tr_b16 a[86:87], v14 offset:9280\n"
"s_xor_b32 s79, s84, 0x280000\n"
"buffer_load_dwordx4 a[168:171], v12, s[60:63], s79 offen\n"
"s_xor_b32 s79, s84, 0x288000\n"
"buffer_load_dwordx4 a[172:175], v12, s[60:63], s79 offen\n"
"s_xor_b32 s79, s84, 0x290000\n"
"buffer_load_dwordx4 a[232:235], v12, s[60:63], s79 offen\n"
"s_xor_b32 s79, s84, 0x298000\n"
"buffer_load_dwordx4 a[236:239], v12, s[60:63], s79 offen\n"
"s_waitcnt vmcnt(40)\n"
"ds_write_b128 v13, a[24:27] offset:17408\n"
"ds_write_b128 v13, a[28:31] offset:18496\n"
"ds_write_b128 v13, a[88:91] offset:19584\n"
"ds_write_b128 v13, a[92:95] offset:20672\n"
"s_waitcnt lgkmcnt(0)\n"
"s_barrier\n"
"ds_read_b64_tr_b16 a[24:25], v14 offset:17408\n"
"ds_read_b64_tr_b16 a[26:27], v14 offset:17952\n"
"ds_read_b64_tr_b16 a[28:29], v14 offset:26112\n"
"ds_read_b64_tr_b16 a[30:31], v14 offset:26656\n"
"ds_read_b64_tr_b16 a[88:89], v14 offset:17440\n"
"ds_read_b64_tr_b16 a[90:91], v14 offset:17984\n"
"ds_read_b64_tr_b16 a[92:93], v14 offset:26144\n"
"ds_read_b64_tr_b16 a[94:95], v14 offset:26688\n"
"s_xor_b32 s79, s84, 0x300000\n"
"buffer_load_dwordx4 a[176:179], v12, s[60:63], s79 offen\n"
"s_xor_b32 s79, s84, 0x308000\n"
"buffer_load_dwordx4 a[180:183], v12, s[60:63], s79 offen\n"
"s_xor_b32 s79, s84, 0x310000\n"
"buffer_load_dwordx4 a[240:243], v12, s[60:63], s79 offen\n"
"s_xor_b32 s79, s84, 0x318000\n"
"buffer_load_dwordx4 a[244:247], v12, s[60:63], s79 offen\n"
"s_waitcnt vmcnt(40)\n"
"ds_write_b128 v13, a[32:35] offset:0\n"
"ds_write_b128 v13, a[36:39] offset:1088\n"
"ds_write_b128 v13, a[96:99] offset:2176\n"
"ds_write_b128 v13, a[100:103] offset:3264\n"
"s_waitcnt lgkmcnt(0)\n"
"s_barrier\n"
"ds_read_b64_tr_b16 a[32:33], v14 offset:0\n"
"ds_read_b64_tr_b16 a[34:35], v14 offset:544\n"
"ds_read_b64_tr_b16 a[36:37], v14 offset:8704\n"
"ds_read_b64_tr_b16 a[38:39], v14 offset:9248\n"
"ds_read_b64_tr_b16 a[96:97], v14 offset:32\n"
"ds_read_b64_tr_b16 a[98:99], v14 offset:576\n"
"ds_read_b64_tr_b16 a[100:101], v14 offset:8736\n"
"ds_read_b64_tr_b16 a[102:103], v14 offset:9280\n"
"s_xor_b32 s79, s84, 0x380000\n"
"buffer_load_dwordx4 a[184:187], v12, s[60:63], s79 offen\n"
"s_xor_b32 s79, s84, 0x388000\n"
"buffer_load_dwordx4 a[188:191], v12, s[60:63], s79 offen\n"
"s_xor_b32 s79, s84, 0x390000\n"
"buffer_load_dwordx4 a[248:251], v12, s[60:63], s79 offen\n"
"s_xor_b32 s79, s84, 0x398000\n"
"buffer_load_dwordx4 a[252:255], v12, s[60:63], s79 offen\n"
"s_waitcnt vmcnt(40)\n"
"ds_write_b128 v13, a[40:43] offset:17408\n"
"ds_write_b128 v13, a[44:47] offset:18496\n"
"ds_write_b128 v13, a[104:107] offset:19584\n"
"ds_write_b128 v13, a[108:111] offset:20672\n"
"s_waitcnt lgkmcnt(0)\n"
"s_barrier\n"
"ds_read_b64_tr_b16 a[40:41], v14 offset:17408\n"
"ds_read_b64_tr_b16 a[42:43], v14 offset:17952\n"
"ds_read_b64_tr_b16 a[44:45], v14 offset:26112\n"
"ds_read_b64_tr_b16 a[46:47], v14 offset:26656\n"
"ds_read_b64_tr_b16 a[104:105], v14 offset:17440\n"
"ds_read_b64_tr_b16 a[106:107], v14 offset:17984\n"
"ds_read_b64_tr_b16 a[108:109], v14 offset:26144\n"
"ds_read_b64_tr_b16 a[110:111], v14 offset:26688\n"
"s_xor_b32 s79, s84, 0x0\n"
"buffer_load_dwordx4 v[32:35], v12, s[64:67], s79 offen\n"
"s_xor_b32 s79, s84, 0x8000\n"
"buffer_load_dwordx4 v[36:39], v12, s[64:67], s79 offen\n"
"s_xor_b32 s79, s84, 0x10000\n"
"buffer_load_dwordx4 v[40:43], v12, s[64:67], s79 offen\n"
"s_xor_b32 s79, s84, 0x18000\n"
"buffer_load_dwordx4 v[44:47], v12, s[64:67], s79 offen\n"
"s_waitcnt vmcnt(40)\n"
"ds_write_b128 v13, a[48:51] offset:0\n"
"ds_write_b128 v13, a[52:55] offset:1088\n"
"ds_write_b128 v13, a[112:115] offset:2176\n"
"ds_write_b128 v13, a[116:119] offset:3264\n"
"s_waitcnt lgkmcnt(0)\n"
"s_barrier\n"
"ds_read_b64_tr_b16 a[48:49], v14 offset:0\n"
"ds_read_b64_tr_b16 a[50:51], v14 offset:544\n"
"ds_read_b64_tr_b16 a[52:53], v14 offset:8704\n"
"ds_read_b64_tr_b16 a[54:55], v14 offset:9248\n"
"ds_read_b64_tr_b16 a[112:113], v14 offset:32\n"
"ds_read_b64_tr_b16 a[114:115], v14 offset:576\n"
"ds_read_b64_tr_b16 a[116:117], v14 offset:8736\n"
"ds_read_b64_tr_b16 a[118:119], v14 offset:9280\n"
"s_xor_b32 s79, s84, 0x80000\n"
"buffer_load_dwordx4 v[48:51], v12, s[64:67], s79 offen\n"
"s_xor_b32 s79, s84, 0x88000\n"
"buffer_load_dwordx4 v[52:55], v12, s[64:67], s79 offen\n"
"s_xor_b32 s79, s84, 0x90000\n"
"buffer_load_dwordx4 v[56:59], v12, s[64:67], s79 offen\n"
"s_xor_b32 s79, s84, 0x98000\n"
"buffer_load_dwordx4 v[60:63], v12, s[64:67], s79 offen\n"
"s_waitcnt vmcnt(40)\n"
"ds_write_b128 v13, a[56:59] offset:17408\n"
"ds_write_b128 v13, a[60:63] offset:18496\n"
"ds_write_b128 v13, a[120:123] offset:19584\n"
"ds_write_b128 v13, a[124:127] offset:20672\n"
"s_waitcnt lgkmcnt(0)\n"
"s_barrier\n"
"ds_read_b64_tr_b16 a[56:57], v14 offset:17408\n"
"ds_read_b64_tr_b16 a[58:59], v14 offset:17952\n"
"ds_read_b64_tr_b16 a[60:61], v14 offset:26112\n"
"ds_read_b64_tr_b16 a[62:63], v14 offset:26656\n"
"ds_read_b64_tr_b16 a[120:121], v14 offset:17440\n"
"ds_read_b64_tr_b16 a[122:123], v14 offset:17984\n"
"ds_read_b64_tr_b16 a[124:125], v14 offset:26144\n"
"ds_read_b64_tr_b16 a[126:127], v14 offset:26688\n"
"s_xor_b32 s79, s84, 0x100000\n"
"buffer_load_dwordx4 v[144:147], v12, s[64:67], s79 offen\n"
"s_xor_b32 s79, s84, 0x108000\n"
"buffer_load_dwordx4 v[148:151], v12, s[64:67], s79 offen\n"
"s_xor_b32 s79, s84, 0x110000\n"
"buffer_load_dwordx4 v[208:211], v12, s[64:67], s79 offen\n"
"s_xor_b32 s79, s84, 0x118000\n"
"buffer_load_dwordx4 v[212:215], v12, s[64:67], s79 offen\n"
"s_waitcnt vmcnt(40)\n"
"ds_write_b128 v13, a[128:131] offset:0\n"
"ds_write_b128 v13, a[132:135] offset:1088\n"
"ds_write_b128 v13, a[192:195] offset:2176\n"
"ds_write_b128 v13, a[196:199] offset:3264\n"
"s_waitcnt lgkmcnt(0)\n"
"s_barrier\n"
"ds_read_b64_tr_b16 a[128:129], v14 offset:0\n"
"ds_read_b64_tr_b16 a[130:131], v14 offset:544\n"
"ds_read_b64_tr_b16 a[132:133], v14 offset:8704\n"
"ds_read_b64_tr_b16 a[134:135], v14 offset:9248\n"
"ds_read_b64_tr_b16 a[192:193], v14 offset:32\n"
"ds_read_b64_tr_b16 a[194:195], v14 offset:576\n"
"ds_read_b64_tr_b16 a[196:197], v14 offset:8736\n"
"ds_read_b64_tr_b16 a[198:199], v14 offset:9280\n"
"s_xor_b32 s79, s84, 0x180000\n"
"buffer_load_dwordx4 v[152:155], v12, s[64:67], s79 offen\n"
"s_xor_b32 s79, s84, 0x188000\n"
"buffer_load_dwordx4 v[156:159], v12, s[64:67], s79 offen\n"
"s_xor_b32 s79, s84, 0x190000\n"
"buffer_load_dwordx4 v[216:219], v12, s[64:67], s79 offen\n"
"s_xor_b32 s79, s84, 0x198000\n"
"buffer_load_dwordx4 v[220:223], v12, s[64:67], s79 offen\n"
"s_waitcnt vmcnt(40)\n"
"ds_write_b128 v13, a[136:139] offset:17408\n"
"ds_write_b128 v13, a[140:143] offset:18496\n"
"ds_write_b128 v13, a[200:203] offset:19584\n"
"ds_write_b128 v13, a[204:207] offset:20672\n"
"s_waitcnt lgkmcnt(0)\n"
"s_barrier\n"
"ds_read_b64_tr_b16 a[136:137], v14 offset:17408\n"
"ds_read_b64_tr_b16 a[138:139], v14 offset:17952\n"
"ds_read_b64_tr_b16 a[140:141], v14 offset:26112\n"
"ds_read_b64_tr_b16 a[142:143], v14 offset:26656\n"
"ds_read_b64_tr_b16 a[200:201], v14 offset:17440\n"
"ds_read_b64_tr_b16 a[202:203], v14 offset:17984\n"
"ds_read_b64_tr_b16 a[204:205], v14 offset:26144\n"
"ds_read_b64_tr_b16 a[206:207], v14 offset:26688\n"
"s_xor_b32 s79, s84, 0x200000\n"
"buffer_load_dwordx4 v[160:163], v12, s[64:67], s79 offen\n"
"s_xor_b32 s79, s84, 0x208000\n"
"buffer_load_dwordx4 v[164:167], v12, s[64:67], s79 offen\n"
"s_xor_b32 s79, s84, 0x210000\n"
"buffer_load_dwordx4 v[224:227], v12, s[64:67], s79 offen\n"
"s_xor_b32 s79, s84, 0x218000\n"
"buffer_load_dwordx4 v[228:231], v12, s[64:67], s79 offen\n"
"s_waitcnt vmcnt(40)\n"
"ds_write_b128 v13, a[144:147] offset:0\n"
"ds_write_b128 v13, a[148:151] offset:1088\n"
"ds_write_b128 v13, a[208:211] offset:2176\n"
"ds_write_b128 v13, a[212:215] offset:3264\n"
"s_waitcnt lgkmcnt(0)\n"
"s_barrier\n"
"ds_read_b64_tr_b16 a[144:145], v14 offset:0\n"
"ds_read_b64_tr_b16 a[146:147], v14 offset:544\n"
"ds_read_b64_tr_b16 a[148:149], v14 offset:8704\n"
"ds_read_b64_tr_b16 a[150:151], v14 offset:9248\n"
"ds_read_b64_tr_b16 a[208:209], v14 offset:32\n"
"ds_read_b64_tr_b16 a[210:211], v14 offset:576\n"
"ds_read_b64_tr_b16 a[212:213], v14 offset:8736\n"
"ds_read_b64_tr_b16 a[214:215], v14 offset:9280\n"
"s_xor_b32 s79, s84, 0x280000\n"
"buffer_load_dwordx4 v[168:171], v12, s[64:67], s79 offen\n"
"s_xor_b32 s79, s84, 0x288000\n"
"buffer_load_dwordx4 v[172:175], v12, s[64:67], s79 offen\n"
"s_xor_b32 s79, s84, 0x290000\n"
"buffer_load_dwordx4 v[232:235], v12, s[64:67], s79 offen\n"
"s_xor_b32 s79, s84, 0x298000\n"
"buffer_load_dwordx4 v[236:239], v12, s[64:67], s79 offen\n"
"s_waitcnt vmcnt(40)\n"
"ds_write_b128 v13, a[152:155] offset:17408\n"
"ds_write_b128 v13, a[156:159] offset:18496\n"
"ds_write_b128 v13, a[216:219] offset:19584\n"
"ds_write_b128 v13, a[220:223] offset:20672\n"
"s_waitcnt lgkmcnt(0)\n"
"s_barrier\n"
"ds_read_b64_tr_b16 a[152:153], v14 offset:17408\n"
"ds_read_b64_tr_b16 a[154:155], v14 offset:17952\n"
"ds_read_b64_tr_b16 a[156:157], v14 offset:26112\n"
"ds_read_b64_tr_b16 a[158:159], v14 offset:26656\n"
"ds_read_b64_tr_b16 a[216:217], v14 offset:17440\n"
"ds_read_b64_tr_b16 a[218:219], v14 offset:17984\n"
"ds_read_b64_tr_b16 a[220:221], v14 offset:26144\n"
"ds_read_b64_tr_b16 a[222:223], v14 offset:26688\n"
"s_xor_b32 s79, s84, 0x300000\n"
"buffer_load_dwordx4 v[176:179], v12, s[64:67], s79 offen\n"
"s_xor_b32 s79, s84, 0x308000\n"
"buffer_load_dwordx4 v[180:183], v12, s[64:67], s79 offen\n"
"s_xor_b32 s79, s84, 0x310000\n"
"buffer_load_dwordx4 v[240:243], v12, s[64:67], s79 offen\n"
"s_xor_b32 s79, s84, 0x318000\n"
"buffer_load_dwordx4 v[244:247], v12, s[64:67], s79 offen\n"
"s_waitcnt vmcnt(40)\n"
"ds_write_b128 v13, a[160:163] offset:0\n"
"ds_write_b128 v13, a[164:167] offset:1088\n"
"ds_write_b128 v13, a[224:227] offset:2176\n"
"ds_write_b128 v13, a[228:231] offset:3264\n"
"s_waitcnt lgkmcnt(0)\n"
"s_barrier\n"
"ds_read_b64_tr_b16 a[160:161], v14 offset:0\n"
"ds_read_b64_tr_b16 a[162:163], v14 offset:544\n"
"ds_read_b64_tr_b16 a[164:165], v14 offset:8704\n"
"ds_read_b64_tr_b16 a[166:167], v14 offset:9248\n"
"ds_read_b64_tr_b16 a[224:225], v14 offset:32\n"
"ds_read_b64_tr_b16 a[226:227], v14 offset:576\n"
"ds_read_b64_tr_b16 a[228:229], v14 offset:8736\n"
"ds_read_b64_tr_b16 a[230:231], v14 offset:9280\n"
"s_xor_b32 s79, s84, 0x380000\n"
"buffer_load_dwordx4 v[184:187], v12, s[64:67], s79 offen\n"
"s_xor_b32 s79, s84, 0x388000\n"
"buffer_load_dwordx4 v[188:191], v12, s[64:67], s79 offen\n"
"s_xor_b32 s79, s84, 0x390000\n"
"buffer_load_dwordx4 v[248:251], v12, s[64:67], s79 offen\n"
"s_xor_b32 s79, s84, 0x398000\n"
"buffer_load_dwordx4 v[252:255], v12, s[64:67], s79 offen\n"
"s_waitcnt vmcnt(40)\n"
"ds_write_b128 v13, a[168:171] offset:17408\n"
"ds_write_b128 v13, a[172:175] offset:18496\n"
"ds_write_b128 v13, a[232:235] offset:19584\n"
"ds_write_b128 v13, a[236:239] offset:20672\n"
"s_waitcnt lgkmcnt(0)\n"
"s_barrier\n"
"ds_read_b64_tr_b16 a[168:169], v14 offset:17408\n"
"ds_read_b64_tr_b16 a[170:171], v14 offset:17952\n"
"ds_read_b64_tr_b16 a[172:173], v14 offset:26112\n"
"ds_read_b64_tr_b16 a[174:175], v14 offset:26656\n"
"ds_read_b64_tr_b16 a[232:233], v14 offset:17440\n"
"ds_read_b64_tr_b16 a[234:235], v14 offset:17984\n"
"ds_read_b64_tr_b16 a[236:237], v14 offset:26144\n"
"ds_read_b64_tr_b16 a[238:239], v14 offset:26688\n"
"s_waitcnt vmcnt(36)\n"
"ds_write_b128 v13, a[176:179] offset:0\n"
"ds_write_b128 v13, a[180:183] offset:1088\n"
"ds_write_b128 v13, a[240:243] offset:2176\n"
"ds_write_b128 v13, a[244:247] offset:3264\n"
"s_waitcnt lgkmcnt(0)\n"
"s_barrier\n"
"ds_read_b64_tr_b16 a[176:177], v14 offset:0\n"
"ds_read_b64_tr_b16 a[178:179], v14 offset:544\n"
"ds_read_b64_tr_b16 a[180:181], v14 offset:8704\n"
"ds_read_b64_tr_b16 a[182:183], v14 offset:9248\n"
"ds_read_b64_tr_b16 a[240:241], v14 offset:32\n"
"ds_read_b64_tr_b16 a[242:243], v14 offset:576\n"
"ds_read_b64_tr_b16 a[244:245], v14 offset:8736\n"
"ds_read_b64_tr_b16 a[246:247], v14 offset:9280\n"
"s_waitcnt vmcnt(32)\n"
"ds_write_b128 v13, a[184:187] offset:17408\n"
"ds_write_b128 v13, a[188:191] offset:18496\n"
"ds_write_b128 v13, a[248:251] offset:19584\n"
"ds_write_b128 v13, a[252:255] offset:20672\n"
"s_waitcnt lgkmcnt(0)\n"
"s_barrier\n"
"ds_read_b64_tr_b16 a[184:185], v14 offset:17408\n"
"ds_read_b64_tr_b16 a[186:187], v14 offset:17952\n"
"ds_read_b64_tr_b16 a[188:189], v14 offset:26112\n"
"ds_read_b64_tr_b16 a[190:191], v14 offset:26656\n"
"ds_read_b64_tr_b16 a[248:249], v14 offset:17440\n"
"ds_read_b64_tr_b16 a[250:251], v14 offset:17984\n"
"ds_read_b64_tr_b16 a[252:253], v14 offset:26144\n"
"ds_read_b64_tr_b16 a[254:255], v14 offset:26688\n"
"s_waitcnt vmcnt(28)\n"
"ds_write_b128 v13, v[32:35] offset:0\n"
"ds_write_b128 v13, v[36:39] offset:1088\n"
"ds_write_b128 v13, v[40:43] offset:2176\n"
"ds_write_b128 v13, v[44:47] offset:3264\n"
"s_waitcnt lgkmcnt(0)\n"
"s_barrier\n"
"ds_read_b64_tr_b16 v[32:33], v14 offset:0\n"
"ds_read_b64_tr_b16 v[34:35], v14 offset:544\n"
"ds_read_b64_tr_b16 v[36:37], v14 offset:8704\n"
"ds_read_b64_tr_b16 v[38:39], v14 offset:9248\n"
"ds_read_b64_tr_b16 v[40:41], v14 offset:32\n"
"ds_read_b64_tr_b16 v[42:43], v14 offset:576\n"
"ds_read_b64_tr_b16 v[44:45], v14 offset:8736\n"
"ds_read_b64_tr_b16 v[46:47], v14 offset:9280\n"
"s_waitcnt vmcnt(24)\n"
"ds_write_b128 v13, v[48:51] offset:17408\n"
"ds_write_b128 v13, v[52:55] offset:18496\n"
"ds_write_b128 v13, v[56:59] offset:19584\n"
"ds_write_b128 v13, v[60:63] offset:20672\n"
"s_waitcnt lgkmcnt(0)\n"
"s_barrier\n"
"ds_read_b64_tr_b16 v[48:49], v14 offset:17408\n"
"ds_read_b64_tr_b16 v[50:51], v14 offset:17952\n"
"ds_read_b64_tr_b16 v[52:53], v14 offset:26112\n"
"ds_read_b64_tr_b16 v[54:55], v14 offset:26656\n"
"ds_read_b64_tr_b16 v[56:57], v14 offset:17440\n"
"ds_read_b64_tr_b16 v[58:59], v14 offset:17984\n"
"ds_read_b64_tr_b16 v[60:61], v14 offset:26144\n"
"ds_read_b64_tr_b16 v[62:63], v14 offset:26688\n"
"s_waitcnt vmcnt(20)\n"
"ds_write_b128 v13, v[144:147] offset:0\n"
"ds_write_b128 v13, v[148:151] offset:1088\n"
"ds_write_b128 v13, v[208:211] offset:2176\n"
"ds_write_b128 v13, v[212:215] offset:3264\n"
"s_waitcnt lgkmcnt(0)\n"
"s_barrier\n"
"ds_read_b64_tr_b16 v[144:145], v14 offset:0\n"
"ds_read_b64_tr_b16 v[146:147], v14 offset:544\n"
"ds_read_b64_tr_b16 v[148:149], v14 offset:8704\n"
"ds_read_b64_tr_b16 v[150:151], v14 offset:9248\n"
"ds_read_b64_tr_b16 v[208:209], v14 offset:32\n"
"ds_read_b64_tr_b16 v[210:211], v14 offset:576\n"
"ds_read_b64_tr_b16 v[212:213], v14 offset:8736\n"
"ds_read_b64_tr_b16 v[214:215], v14 offset:9280\n"
"s_waitcnt vmcnt(16)\n"
"ds_write_b128 v13, v[152:155] offset:17408\n"
"ds_write_b128 v13, v[156:159] offset:18496\n"
"ds_write_b128 v13, v[216:219] offset:19584\n"
"ds_write_b128 v13, v[220:223] offset:20672\n"
"s_waitcnt lgkmcnt(0)\n"
"s_barrier\n"
"ds_read_b64_tr_b16 v[152:153], v14 offset:17408\n"
"ds_read_b64_tr_b16 v[154:155], v14 offset:17952\n"
"ds_read_b64_tr_b16 v[156:157], v14 offset:26112\n"
"ds_read_b64_tr_b16 v[158:159], v14 offset:26656\n"
"ds_read_b64_tr_b16 v[216:217], v14 offset:17440\n"
"ds_read_b64_tr_b16 v[218:219], v14 offset:17984\n"
"ds_read_b64_tr_b16 v[220:221], v14 offset:26144\n"
"ds_read_b64_tr_b16 v[222:223], v14 offset:26688\n"
"s_waitcnt vmcnt(12)\n"
"ds_write_b128 v13, v[160:163] offset:0\n"
"ds_write_b128 v13, v[164:167] offset:1088\n"
"ds_write_b128 v13, v[224:227] offset:2176\n"
"ds_write_b128 v13, v[228:231] offset:3264\n"
"s_waitcnt lgkmcnt(0)\n"
"s_barrier\n"
"ds_read_b64_tr_b16 v[160:161], v14 offset:0\n"
"ds_read_b64_tr_b16 v[162:163], v14 offset:544\n"
"ds_read_b64_tr_b16 v[164:165], v14 offset:8704\n"
"ds_read_b64_tr_b16 v[166:167], v14 offset:9248\n"
"ds_read_b64_tr_b16 v[224:225], v14 offset:32\n"
"ds_read_b64_tr_b16 v[226:227], v14 offset:576\n"
"ds_read_b64_tr_b16 v[228:229], v14 offset:8736\n"
"ds_read_b64_tr_b16 v[230:231], v14 offset:9280\n"
"s_waitcnt vmcnt(8)\n"
"ds_write_b128 v13, v[168:171] offset:17408\n"
"ds_write_b128 v13, v[172:175] offset:18496\n"
"ds_write_b128 v13, v[232:235] offset:19584\n"
"ds_write_b128 v13, v[236:239] offset:20672\n"
"s_waitcnt lgkmcnt(0)\n"
"s_barrier\n"
"ds_read_b64_tr_b16 v[168:169], v14 offset:17408\n"
"ds_read_b64_tr_b16 v[170:171], v14 offset:17952\n"
"ds_read_b64_tr_b16 v[172:173], v14 offset:26112\n"
"ds_read_b64_tr_b16 v[174:175], v14 offset:26656\n"
"ds_read_b64_tr_b16 v[232:233], v14 offset:17440\n"
"ds_read_b64_tr_b16 v[234:235], v14 offset:17984\n"
"ds_read_b64_tr_b16 v[236:237], v14 offset:26144\n"
"ds_read_b64_tr_b16 v[238:239], v14 offset:26688\n"
"s_waitcnt vmcnt(4)\n"
"ds_write_b128 v13, v[176:179] offset:0\n"
"ds_write_b128 v13, v[180:183] offset:1088\n"
"ds_write_b128 v13, v[240:243] offset:2176\n"
"ds_write_b128 v13, v[244:247] offset:3264\n"
"s_waitcnt lgkmcnt(0)\n"
"s_barrier\n"
"s_mov_b32 s79, 0x1b0200\n"
"buffer_load_dwordx2 v[104:105], off, s[52:55], s79 sc1\n"
"s_sub_u32 s64, s34, 0x1a0000\n"
"s_lshl_b32 s64, s64, 5\n"
"s_add_u32 s64, s64, 0x1b2000\n"
"buffer_load_dwordx2 v[106:107], off, s[52:55], s64 sc1\n"
"ds_read_b64_tr_b16 v[176:177], v14 offset:0\n"
"ds_read_b64_tr_b16 v[178:179], v14 offset:544\n"
"ds_read_b64_tr_b16 v[180:181], v14 offset:8704\n"
"ds_read_b64_tr_b16 v[182:183], v14 offset:9248\n"
"ds_read_b64_tr_b16 v[240:241], v14 offset:32\n"
"ds_read_b64_tr_b16 v[242:243], v14 offset:576\n"
"ds_read_b64_tr_b16 v[244:245], v14 offset:8736\n"
"ds_read_b64_tr_b16 v[246:247], v14 offset:9280\n"
"s_waitcnt vmcnt(2)\n"
"ds_write_b128 v13, v[184:187] offset:17408\n"
"ds_write_b128 v13, v[188:191] offset:18496\n"
"ds_write_b128 v13, v[248:251] offset:19584\n"
"ds_write_b128 v13, v[252:255] offset:20672\n"
"s_waitcnt lgkmcnt(0)\n"
"s_barrier\n"
"ds_read_b64_tr_b16 v[184:185], v14 offset:17408\n"
"ds_read_b64_tr_b16 v[186:187], v14 offset:17952\n"
"ds_read_b64_tr_b16 v[188:189], v14 offset:26112\n"
"ds_read_b64_tr_b16 v[190:191], v14 offset:26656\n"
"ds_read_b64_tr_b16 v[248:249], v14 offset:17440\n"
"ds_read_b64_tr_b16 v[250:251], v14 offset:17984\n"
"ds_read_b64_tr_b16 v[252:253], v14 offset:26144\n"
"ds_read_b64_tr_b16 v[254:255], v14 offset:26688\n"
"s_waitcnt lgkmcnt(0)\n"
"ds_write_b128 v11, v[32:35] offset:8192\n"
"ds_write_b128 v11, v[36:39] offset:9216\n"
"ds_write_b128 v11, v[40:43] offset:10240\n"
"ds_write_b128 v11, v[44:47] offset:11264\n"
"ds_write_b128 v11, v[48:51] offset:12288\n"
"ds_write_b128 v11, v[52:55] offset:13312\n"
"ds_write_b128 v11, v[56:59] offset:14336\n"
"ds_write_b128 v11, v[60:63] offset:15360\n"
"s_branch L_sudone_%=\n"
"L_su1_%=:\n"
"s_xor_b32 s79, s84, 0x0\n"
"buffer_load_dwordx4 a[24:27], v12, s[56:59], s79 offen\n"
"s_xor_b32 s79, s84, 0x8000\n"
"buffer_load_dwordx4 a[28:31], v12, s[56:59], s79 offen\n"
"s_xor_b32 s79, s84, 0x10000\n"
"buffer_load_dwordx4 a[88:91], v12, s[56:59], s79 offen\n"
"s_xor_b32 s79, s84, 0x18000\n"
"buffer_load_dwordx4 a[92:95], v12, s[56:59], s79 offen\n"
"s_xor_b32 s79, s84, 0x80000\n"
"buffer_load_dwordx4 a[0:3], v12, s[56:59], s79 offen\n"
"s_xor_b32 s79, s84, 0x88000\n"
"buffer_load_dwordx4 a[4:7], v12, s[56:59], s79 offen\n"
"s_xor_b32 s79, s84, 0x90000\n"
"buffer_load_dwordx4 a[64:67], v12, s[56:59], s79 offen\n"
"s_xor_b32 s79, s84, 0x98000\n"
"buffer_load_dwordx4 a[68:71], v12, s[56:59], s79 offen\n"
"s_xor_b32 s79, s84, 0x100000\n"
"buffer_load_dwordx4 a[8:11], v12, s[56:59], s79 offen\n"
"s_xor_b32 s79, s84, 0x108000\n"
"buffer_load_dwordx4 a[12:15], v12, s[56:59], s79 offen\n"
"s_xor_b32 s79, s84, 0x110000\n"
"buffer_load_dwordx4 a[72:75], v12, s[56:59], s79 offen\n"
"s_xor_b32 s79, s84, 0x118000\n"
"buffer_load_dwordx4 a[76:79], v12, s[56:59], s79 offen\n"
"s_xor_b32 s79, s84, 0x180000\n"
"buffer_load_dwordx4 a[16:19], v12, s[56:59], s79 offen\n"
"s_xor_b32 s79, s84, 0x188000\n"
"buffer_load_dwordx4 a[20:23], v12, s[56:59], s79 offen\n"
"s_xor_b32 s79, s84, 0x190000\n"
"buffer_load_dwordx4 a[80:83], v12, s[56:59], s79 offen\n"
"s_xor_b32 s79, s84, 0x198000\n"
"buffer_load_dwordx4 a[84:87], v12, s[56:59], s79 offen\n"
"s_xor_b32 s79, s84, 0x200000\n"
"buffer_load_dwordx4 a[56:59], v12, s[56:59], s79 offen\n"
"s_xor_b32 s79, s84, 0x208000\n"
"buffer_load_dwordx4 a[60:63], v12, s[56:59], s79 offen\n"
"s_xor_b32 s79, s84, 0x210000\n"
"buffer_load_dwordx4 a[120:123], v12, s[56:59], s79 offen\n"
"s_xor_b32 s79, s84, 0x218000\n"
"buffer_load_dwordx4 a[124:127], v12, s[56:59], s79 offen\n"
"s_xor_b32 s79, s84, 0x280000\n"
"buffer_load_dwordx4 a[32:35], v12, s[56:59], s79 offen\n"
"s_xor_b32 s79, s84, 0x288000\n"
"buffer_load_dwordx4 a[36:39], v12, s[56:59], s79 offen\n"
"s_xor_b32 s79, s84, 0x290000\n"
"buffer_load_dwordx4 a[96:99], v12, s[56:59], s79 offen\n"
"s_xor_b32 s79, s84, 0x298000\n"
"buffer_load_dwordx4 a[100:103], v12, s[56:59], s79 offen\n"
"s_xor_b32 s79, s84, 0x300000\n"
"buffer_load_dwordx4 a[40:43], v12, s[56:59], s79 offen\n"
"s_xor_b32 s79, s84, 0x308000\n"
"buffer_load_dwordx4 a[44:47], v12, s[56:59], s79 offen\n"
"s_xor_b32 s79, s84, 0x310000\n"
"buffer_load_dwordx4 a[104:107], v12, s[56:59], s79 offen\n"
"s_xor_b32 s79, s84, 0x318000\n"
"buffer_load_dwordx4 a[108:111], v12, s[56:59], s79 offen\n"
"s_xor_b32 s79, s84, 0x380000\n"
"buffer_load_dwordx4 a[48:51], v12, s[56:59], s79 offen\n"
"s_xor_b32 s79, s84, 0x388000\n"
"buffer_load_dwordx4 a[52:55], v12, s[56:59], s79 offen\n"
"s_xor_b32 s79, s84, 0x390000\n"
"buffer_load_dwordx4 a[112:115], v12, s[56:59], s79 offen\n"
"s_xor_b32 s79, s84, 0x398000\n"
"buffer_load_dwordx4 a[116:119], v12, s[56:59], s79 offen\n"
"s_xor_b32 s79, s84, 0x0\n"
"buffer_load_dwordx4 a[152:155], v12, s[60:63], s79 offen\n"
"s_xor_b32 s79, s84, 0x8000\n"
"buffer_load_dwordx4 a[156:159], v12, s[60:63], s79 offen\n"
"s_xor_b32 s79, s84, 0x10000\n"
"buffer_load_dwordx4 a[216:219], v12, s[60:63], s79 offen\n"
"s_xor_b32 s79, s84, 0x18000\n"
"buffer_load_dwordx4 a[220:223], v12, s[60:63], s79 offen\n"
"s_xor_b32 s79, s84, 0x80000\n"
"buffer_load_dwordx4 a[128:131], v12, s[60:63], s79 offen\n"
"s_xor_b32 s79, s84, 0x88000\n"
"buffer_load_dwordx4 a[132:135], v12, s[60:63], s79 offen\n"
"s_xor_b32 s79, s84, 0x90000\n"
"buffer_load_dwordx4 a[192:195], v12, s[60:63], s79 offen\n"
"s_xor_b32 s79, s84, 0x98000\n"
"buffer_load_dwordx4 a[196:199], v12, s[60:63], s79 offen\n"
"s_waitcnt vmcnt(40)\n"
"ds_write_b128 v11, v[32:35] offset:0\n"
"ds_write_b128 v11, v[36:39] offset:1024\n"
"ds_write_b128 v11, v[40:43] offset:2048\n"
"ds_write_b128 v11, v[44:47] offset:3072\n"
"ds_write_b128 v11, v[48:51] offset:4096\n"
"ds_write_b128 v11, v[52:55] offset:5120\n"
"ds_write_b128 v11, v[56:59] offset:6144\n"
"ds_write_b128 v11, v[60:63] offset:7168\n"
"ds_write_b128 v11, v[64:67] offset:8192\n"
"ds_write_b128 v11, v[68:71] offset:9216\n"
"ds_write_b128 v11, v[72:75] offset:10240\n"
"ds_write_b128 v11, v[76:79] offset:11264\n"
"ds_write_b128 v11, v[80:83] offset:12288\n"
"ds_write_b128 v11, v[84:87] offset:13312\n"
"ds_write_b128 v11, v[88:91] offset:14336\n"
"ds_write_b128 v11, v[92:95] offset:15360\n"
"ds_read_b64_tr_b16 v[128:129], v16 offset:0\n"
"ds_read_b64_tr_b16 v[130:131], v16 offset:256\n"
"ds_read_b64_tr_b16 v[132:133], v16 offset:4096\n"
"ds_read_b64_tr_b16 v[134:135], v16 offset:4352\n"
"ds_read_b64_tr_b16 v[136:137], v16 offset:32\n"
"ds_read_b64_tr_b16 v[138:139], v16 offset:288\n"
"ds_read_b64_tr_b16 v[140:141], v16 offset:4128\n"
"ds_read_b64_tr_b16 v[142:143], v16 offset:4384\n"
"ds_read_b64_tr_b16 v[32:33], v16 offset:64\n"
"ds_read_b64_tr_b16 v[34:35], v16 offset:320\n"
"ds_read_b64_tr_b16 v[36:37], v16 offset:4160\n"
"ds_read_b64_tr_b16 v[38:39], v16 offset:4416\n"
"ds_read_b64_tr_b16 v[40:41], v16 offset:96\n"
"ds_read_b64_tr_b16 v[42:43], v16 offset:352\n"
"ds_read_b64_tr_b16 v[44:45], v16 offset:4192\n"
"ds_read_b64_tr_b16 v[46:47], v16 offset:4448\n"
"ds_read_b64_tr_b16 v[192:193], v16 offset:8192\n"
"ds_read_b64_tr_b16 v[194:195], v16 offset:8448\n"
"ds_read_b64_tr_b16 v[196:197], v16 offset:12288\n"
"ds_read_b64_tr_b16 v[198:199], v16 offset:12544\n"
"ds_read_b64_tr_b16 v[200:201], v16 offset:8224\n"
"ds_read_b64_tr_b16 v[202:203], v16 offset:8480\n"
"ds_read_b64_tr_b16 v[204:205], v16 offset:12320\n"
"ds_read_b64_tr_b16 v[206:207], v16 offset:12576\n"
"ds_read_b64_tr_b16 v[48:49], v16 offset:8256\n"
"ds_read_b64_tr_b16 v[50:51], v16 offset:8512\n"
"ds_read_b64_tr_b16 v[52:53], v16 offset:12352\n"
"ds_read_b64_tr_b16 v[54:55], v16 offset:12608\n"
"ds_read_b64_tr_b16 v[56:57], v16 offset:8288\n"
"ds_read_b64_tr_b16 v[58:59], v16 offset:8544\n"
"ds_read_b64_tr_b16 v[60:61], v16 offset:12384\n"
"ds_read_b64_tr_b16 v[62:63], v16 offset:12640\n"
"s_waitcnt lgkmcnt(0)\n"
"ds_write_b128 v11, v[32:35] offset:0\n"
"ds_write_b128 v11, v[36:39] offset:1024\n"
"ds_write_b128 v11, v[40:43] offset:2048\n"
"ds_write_b128 v11, v[44:47] offset:3072\n"
"ds_write_b128 v11, v[48:51] offset:4096\n"
"ds_write_b128 v11, v[52:55] offset:5120\n"
"ds_write_b128 v11, v[56:59] offset:6144\n"
"ds_write_b128 v11, v[60:63] offset:7168\n"
"s_xor_b32 s79, s84, 0x100000\n"
"buffer_load_dwordx4 a[136:139], v12, s[60:63], s79 offen\n"
"s_xor_b32 s79, s84, 0x108000\n"
"buffer_load_dwordx4 a[140:143], v12, s[60:63], s79 offen\n"
"s_xor_b32 s79, s84, 0x110000\n"
"buffer_load_dwordx4 a[200:203], v12, s[60:63], s79 offen\n"
"s_xor_b32 s79, s84, 0x118000\n"
"buffer_load_dwordx4 a[204:207], v12, s[60:63], s79 offen\n"
"s_waitcnt vmcnt(40)\n"
"ds_write_b128 v13, a[24:27] offset:0\n"
"ds_write_b128 v13, a[28:31] offset:1088\n"
"ds_write_b128 v13, a[88:91] offset:2176\n"
"ds_write_b128 v13, a[92:95] offset:3264\n"
"s_waitcnt lgkmcnt(0)\n"
"s_barrier\n"
"ds_read_b64_tr_b16 a[24:25], v14 offset:0\n"
"ds_read_b64_tr_b16 a[26:27], v14 offset:544\n"
"ds_read_b64_tr_b16 a[28:29], v14 offset:8704\n"
"ds_read_b64_tr_b16 a[30:31], v14 offset:9248\n"
"ds_read_b64_tr_b16 a[88:89], v14 offset:32\n"
"ds_read_b64_tr_b16 a[90:91], v14 offset:576\n"
"ds_read_b64_tr_b16 a[92:93], v14 offset:8736\n"
"ds_read_b64_tr_b16 a[94:95], v14 offset:9280\n"
"s_xor_b32 s79, s84, 0x180000\n"
"buffer_load_dwordx4 a[144:147], v12, s[60:63], s79 offen\n"
"s_xor_b32 s79, s84, 0x188000\n"
"buffer_load_dwordx4 a[148:151], v12, s[60:63], s79 offen\n"
"s_xor_b32 s79, s84, 0x190000\n"
"buffer_load_dwordx4 a[208:211], v12, s[60:63], s79 offen\n"
"s_xor_b32 s79, s84, 0x198000\n"
"buffer_load_dwordx4 a[212:215], v12, s[60:63], s79 offen\n"
"s_waitcnt vmcnt(40)\n"
"ds_write_b128 v13, a[0:3] offset:17408\n"
"ds_write_b128 v13, a[4:7] offset:18496\n"
"ds_write_b128 v13, a[64:67] offset:19584\n"
"ds_write_b128 v13, a[68:71] offset:20672\n"
"s_waitcnt lgkmcnt(0)\n"
"s_barrier\n"
"ds_read_b64_tr_b16 a[0:1], v14 offset:17408\n"
"ds_read_b64_tr_b16 a[2:3], v14 offset:17952\n"
"ds_read_b64_tr_b16 a[4:5], v14 offset:26112\n"
"ds_read_b64_tr_b16 a[6:7], v14 offset:26656\n"
"ds_read_b64_tr_b16 a[64:65], v14 offset:17440\n"
"ds_read_b64_tr_b16 a[66:67], v14 offset:17984\n"
"ds_read_b64_tr_b16 a[68:69], v14 offset:26144\n"
"ds_read_b64_tr_b16 a[70:71], v14 offset:26688\n"
"s_xor_b32 s79, s84, 0x200000\n"
"buffer_load_dwordx4 a[184:187], v12, s[60:63], s79 offen\n"
"s_xor_b32 s79, s84, 0x208000\n"
"buffer_load_dwordx4 a[188:191], v12, s[60:63], s79 offen\n"
"s_xor_b32 s79, s84, 0x210000\n"
"buffer_load_dwordx4 a[248:251], v12, s[60:63], s79 offen\n"
"s_xor_b32 s79, s84, 0x218000\n"
"buffer_load_dwordx4 a[252:255], v12, s[60:63], s79 offen\n"
"s_waitcnt vmcnt(40)\n"
"ds_write_b128 v13, a[8:11] offset:0\n"
"ds_write_b128 v13, a[12:15] offset:1088\n"
"ds_write_b128 v13, a[72:75] offset:2176\n"
"ds_write_b128 v13, a[76:79] offset:3264\n"
"s_waitcnt lgkmcnt(0)\n"
"s_barrier\n"
"ds_read_b64_tr_b16 a[8:9], v14 offset:0\n"
"ds_read_b64_tr_b16 a[10:11], v14 offset:544\n"
"ds_read_b64_tr_b16 a[12:13], v14 offset:8704\n"
"ds_read_b64_tr_b16 a[14:15], v14 offset:9248\n"
"ds_read_b64_tr_b16 a[72:73], v14 offset:32\n"
"ds_read_b64_tr_b16 a[74:75], v14 offset:576\n"
"ds_read_b64_tr_b16 a[76:77], v14 offset:8736\n"
"ds_read_b64_tr_b16 a[78:79], v14 offset:9280\n"
"s_xor_b32 s79, s84, 0x280000\n"
"buffer_load_dwordx4 a[160:163], v12, s[60:63], s79 offen\n"
"s_xor_b32 s79, s84, 0x288000\n"
"buffer_load_dwordx4 a[164:167], v12, s[60:63], s79 offen\n"
"s_xor_b32 s79, s84, 0x290000\n"
"buffer_load_dwordx4 a[224:227], v12, s[60:63], s79 offen\n"
"s_xor_b32 s79, s84, 0x298000\n"
"buffer_load_dwordx4 a[228:231], v12, s[60:63], s79 offen\n"
"s_waitcnt vmcnt(40)\n"
"ds_write_b128 v13, a[16:19] offset:17408\n"
"ds_write_b128 v13, a[20:23] offset:18496\n"
"ds_write_b128 v13, a[80:83] offset:19584\n"
"ds_write_b128 v13, a[84:87] offset:20672\n"
"s_waitcnt lgkmcnt(0)\n"
"s_barrier\n"
"ds_read_b64_tr_b16 a[16:17], v14 offset:17408\n"
"ds_read_b64_tr_b16 a[18:19], v14 offset:17952\n"
"ds_read_b64_tr_b16 a[20:21], v14 offset:26112\n"
"ds_read_b64_tr_b16 a[22:23], v14 offset:26656\n"
"ds_read_b64_tr_b16 a[80:81], v14 offset:17440\n"
"ds_read_b64_tr_b16 a[82:83], v14 offset:17984\n"
"ds_read_b64_tr_b16 a[84:85], v14 offset:26144\n"
"ds_read_b64_tr_b16 a[86:87], v14 offset:26688\n"
"s_xor_b32 s79, s84, 0x300000\n"
"buffer_load_dwordx4 a[168:171], v12, s[60:63], s79 offen\n"
"s_xor_b32 s79, s84, 0x308000\n"
"buffer_load_dwordx4 a[172:175], v12, s[60:63], s79 offen\n"
"s_xor_b32 s79, s84, 0x310000\n"
"buffer_load_dwordx4 a[232:235], v12, s[60:63], s79 offen\n"
"s_xor_b32 s79, s84, 0x318000\n"
"buffer_load_dwordx4 a[236:239], v12, s[60:63], s79 offen\n"
"s_waitcnt vmcnt(40)\n"
"ds_write_b128 v13, a[56:59] offset:0\n"
"ds_write_b128 v13, a[60:63] offset:1088\n"
"ds_write_b128 v13, a[120:123] offset:2176\n"
"ds_write_b128 v13, a[124:127] offset:3264\n"
"s_waitcnt lgkmcnt(0)\n"
"s_barrier\n"
"ds_read_b64_tr_b16 a[56:57], v14 offset:0\n"
"ds_read_b64_tr_b16 a[58:59], v14 offset:544\n"
"ds_read_b64_tr_b16 a[60:61], v14 offset:8704\n"
"ds_read_b64_tr_b16 a[62:63], v14 offset:9248\n"
"ds_read_b64_tr_b16 a[120:121], v14 offset:32\n"
"ds_read_b64_tr_b16 a[122:123], v14 offset:576\n"
"ds_read_b64_tr_b16 a[124:125], v14 offset:8736\n"
"ds_read_b64_tr_b16 a[126:127], v14 offset:9280\n"
"s_xor_b32 s79, s84, 0x380000\n"
"buffer_load_dwordx4 a[176:179], v12, s[60:63], s79 offen\n"
"s_xor_b32 s79, s84, 0x388000\n"
"buffer_load_dwordx4 a[180:183], v12, s[60:63], s79 offen\n"
"s_xor_b32 s79, s84, 0x390000\n"
"buffer_load_dwordx4 a[240:243], v12, s[60:63], s79 offen\n"
"s_xor_b32 s79, s84, 0x398000\n"
"buffer_load_dwordx4 a[244:247], v12, s[60:63], s79 offen\n"
"s_waitcnt vmcnt(40)\n"
"ds_write_b128 v13, a[32:35] offset:17408\n"
"ds_write_b128 v13, a[36:39] offset:18496\n"
"ds_write_b128 v13, a[96:99] offset:19584\n"
"ds_write_b128 v13, a[100:103] offset:20672\n"
"s_waitcnt lgkmcnt(0)\n"
"s_barrier\n"
"ds_read_b64_tr_b16 a[32:33], v14 offset:17408\n"
"ds_read_b64_tr_b16 a[34:35], v14 offset:17952\n"
"ds_read_b64_tr_b16 a[36:37], v14 offset:26112\n"
"ds_read_b64_tr_b16 a[38:39], v14 offset:26656\n"
"ds_read_b64_tr_b16 a[96:97], v14 offset:17440\n"
"ds_read_b64_tr_b16 a[98:99], v14 offset:17984\n"
"ds_read_b64_tr_b16 a[100:101], v14 offset:26144\n"
"ds_read_b64_tr_b16 a[102:103], v14 offset:26688\n"
"s_xor_b32 s79, s84, 0x0\n"
"buffer_load_dwordx4 v[152:155], v12, s[64:67], s79 offen\n"
"s_xor_b32 s79, s84, 0x8000\n"
"buffer_load_dwordx4 v[156:159], v12, s[64:67], s79 offen\n"
"s_xor_b32 s79, s84, 0x10000\n"
"buffer_load_dwordx4 v[216:219], v12, s[64:67], s79 offen\n"
"s_xor_b32 s79, s84, 0x18000\n"
"buffer_load_dwordx4 v[220:223], v12, s[64:67], s79 offen\n"
"s_waitcnt vmcnt(40)\n"
"ds_write_b128 v13, a[40:43] offset:0\n"
"ds_write_b128 v13, a[44:47] offset:1088\n"
"ds_write_b128 v13, a[104:107] offset:2176\n"
"ds_write_b128 v13, a[108:111] offset:3264\n"
"s_waitcnt lgkmcnt(0)\n"
"s_barrier\n"
"ds_read_b64_tr_b16 a[40:41], v14 offset:0\n"
"ds_read_b64_tr_b16 a[42:43], v14 offset:544\n"
"ds_read_b64_tr_b16 a[44:45], v14 offset:8704\n"
"ds_read_b64_tr_b16 a[46:47], v14 offset:9248\n"
"ds_read_b64_tr_b16 a[104:105], v14 offset:32\n"
"ds_read_b64_tr_b16 a[106:107], v14 offset:576\n"
"ds_read_b64_tr_b16 a[108:109], v14 offset:8736\n"
"ds_read_b64_tr_b16 a[110:111], v14 offset:9280\n"
"s_xor_b32 s79, s84, 0x80000\n"
"buffer_load_dwordx4 v[32:35], v12, s[64:67], s79 offen\n"
"s_xor_b32 s79, s84, 0x88000\n"
"buffer_load_dwordx4 v[36:39], v12, s[64:67], s79 offen\n"
"s_xor_b32 s79, s84, 0x90000\n"
"buffer_load_dwordx4 v[40:43], v12, s[64:67], s79 offen\n"
"s_xor_b32 s79, s84, 0x98000\n"
"buffer_load_dwordx4 v[44:47], v12, s[64:67], s79 offen\n"
"s_waitcnt vmcnt(40)\n"
"ds_write_b128 v13, a[48:51] offset:17408\n"
"ds_write_b128 v13, a[52:55] offset:18496\n"
"ds_write_b128 v13, a[112:115] offset:19584\n"
"ds_write_b128 v13, a[116:119] offset:20672\n"
"s_waitcnt lgkmcnt(0)\n"
"s_barrier\n"
"ds_read_b64_tr_b16 a[48:49], v14 offset:17408\n"
"ds_read_b64_tr_b16 a[50:51], v14 offset:17952\n"
"ds_read_b64_tr_b16 a[52:53], v14 offset:26112\n"
"ds_read_b64_tr_b16 a[54:55], v14 offset:26656\n"
"ds_read_b64_tr_b16 a[112:113], v14 offset:17440\n"
"ds_read_b64_tr_b16 a[114:115], v14 offset:17984\n"
"ds_read_b64_tr_b16 a[116:117], v14 offset:26144\n"
"ds_read_b64_tr_b16 a[118:119], v14 offset:26688\n"
"s_xor_b32 s79, s84, 0x100000\n"
"buffer_load_dwordx4 v[48:51], v12, s[64:67], s79 offen\n"
"s_xor_b32 s79, s84, 0x108000\n"
"buffer_load_dwordx4 v[52:55], v12, s[64:67], s79 offen\n"
"s_xor_b32 s79, s84, 0x110000\n"
"buffer_load_dwordx4 v[56:59], v12, s[64:67], s79 offen\n"
"s_xor_b32 s79, s84, 0x118000\n"
"buffer_load_dwordx4 v[60:63], v12, s[64:67], s79 offen\n"
"s_waitcnt vmcnt(40)\n"
"ds_write_b128 v13, a[152:155] offset:0\n"
"ds_write_b128 v13, a[156:159] offset:1088\n"
"ds_write_b128 v13, a[216:219] offset:2176\n"
"ds_write_b128 v13, a[220:223] offset:3264\n"
"s_waitcnt lgkmcnt(0)\n"
"s_barrier\n"
"ds_read_b64_tr_b16 a[152:153], v14 offset:0\n"
"ds_read_b64_tr_b16 a[154:155], v14 offset:544\n"
"ds_read_b64_tr_b16 a[156:157], v14 offset:8704\n"
"ds_read_b64_tr_b16 a[158:159], v14 offset:9248\n"
"ds_read_b64_tr_b16 a[216:217], v14 offset:32\n"
"ds_read_b64_tr_b16 a[218:219], v14 offset:576\n"
"ds_read_b64_tr_b16 a[220:221], v14 offset:8736\n"
"ds_read_b64_tr_b16 a[222:223], v14 offset:9280\n"
"s_xor_b32 s79, s84, 0x180000\n"
"buffer_load_dwordx4 v[144:147], v12, s[64:67], s79 offen\n"
"s_xor_b32 s79, s84, 0x188000\n"
"buffer_load_dwordx4 v[148:151], v12, s[64:67], s79 offen\n"
"s_xor_b32 s79, s84, 0x190000\n"
"buffer_load_dwordx4 v[208:211], v12, s[64:67], s79 offen\n"
"s_xor_b32 s79, s84, 0x198000\n"
"buffer_load_dwordx4 v[212:215], v12, s[64:67], s79 offen\n"
"s_waitcnt vmcnt(40)\n"
"ds_write_b128 v13, a[128:131] offset:17408\n"
"ds_write_b128 v13, a[132:135] offset:18496\n"
"ds_write_b128 v13, a[192:195] offset:19584\n"
"ds_write_b128 v13, a[196:199] offset:20672\n"
"s_waitcnt lgkmcnt(0)\n"
"s_barrier\n"
"ds_read_b64_tr_b16 a[128:129], v14 offset:17408\n"
"ds_read_b64_tr_b16 a[130:131], v14 offset:17952\n"
"ds_read_b64_tr_b16 a[132:133], v14 offset:26112\n"
"ds_read_b64_tr_b16 a[134:135], v14 offset:26656\n"
"ds_read_b64_tr_b16 a[192:193], v14 offset:17440\n"
"ds_read_b64_tr_b16 a[194:195], v14 offset:17984\n"
"ds_read_b64_tr_b16 a[196:197], v14 offset:26144\n"
"ds_read_b64_tr_b16 a[198:199], v14 offset:26688\n"
"s_xor_b32 s79, s84, 0x200000\n"
"buffer_load_dwordx4 v[184:187], v12, s[64:67], s79 offen\n"
"s_xor_b32 s79, s84, 0x208000\n"
"buffer_load_dwordx4 v[188:191], v12, s[64:67], s79 offen\n"
"s_xor_b32 s79, s84, 0x210000\n"
"buffer_load_dwordx4 v[248:251], v12, s[64:67], s79 offen\n"
"s_xor_b32 s79, s84, 0x218000\n"
"buffer_load_dwordx4 v[252:255], v12, s[64:67], s79 offen\n"
"s_waitcnt vmcnt(40)\n"
"ds_write_b128 v13, a[136:139] offset:0\n"
"ds_write_b128 v13, a[140:143] offset:1088\n"
"ds_write_b128 v13, a[200:203] offset:2176\n"
"ds_write_b128 v13, a[204:207] offset:3264\n"
"s_waitcnt lgkmcnt(0)\n"
"s_barrier\n"
"ds_read_b64_tr_b16 a[136:137], v14 offset:0\n"
"ds_read_b64_tr_b16 a[138:139], v14 offset:544\n"
"ds_read_b64_tr_b16 a[140:141], v14 offset:8704\n"
"ds_read_b64_tr_b16 a[142:143], v14 offset:9248\n"
"ds_read_b64_tr_b16 a[200:201], v14 offset:32\n"
"ds_read_b64_tr_b16 a[202:203], v14 offset:576\n"
"ds_read_b64_tr_b16 a[204:205], v14 offset:8736\n"
"ds_read_b64_tr_b16 a[206:207], v14 offset:9280\n"
"s_xor_b32 s79, s84, 0x280000\n"
"buffer_load_dwordx4 v[160:163], v12, s[64:67], s79 offen\n"
"s_xor_b32 s79, s84, 0x288000\n"
"buffer_load_dwordx4 v[164:167], v12, s[64:67], s79 offen\n"
"s_xor_b32 s79, s84, 0x290000\n"
"buffer_load_dwordx4 v[224:227], v12, s[64:67], s79 offen\n"
"s_xor_b32 s79, s84, 0x298000\n"
"buffer_load_dwordx4 v[228:231], v12, s[64:67], s79 offen\n"
"s_waitcnt vmcnt(40)\n"
"ds_write_b128 v13, a[144:147] offset:17408\n"
"ds_write_b128 v13, a[148:151] offset:18496\n"
"ds_write_b128 v13, a[208:211] offset:19584\n"
"ds_write_b128 v13, a[212:215] offset:20672\n"
"s_waitcnt lgkmcnt(0)\n"
"s_barrier\n"
"ds_read_b64_tr_b16 a[144:145], v14 offset:17408\n"
"ds_read_b64_tr_b16 a[146:147], v14 offset:17952\n"
"ds_read_b64_tr_b16 a[148:149], v14 offset:26112\n"
"ds_read_b64_tr_b16 a[150:151], v14 offset:26656\n"
"ds_read_b64_tr_b16 a[208:209], v14 offset:17440\n"
"ds_read_b64_tr_b16 a[210:211], v14 offset:17984\n"
"ds_read_b64_tr_b16 a[212:213], v14 offset:26144\n"
"ds_read_b64_tr_b16 a[214:215], v14 offset:26688\n"
"s_xor_b32 s79, s84, 0x300000\n"
"buffer_load_dwordx4 v[168:171], v12, s[64:67], s79 offen\n"
"s_xor_b32 s79, s84, 0x308000\n"
"buffer_load_dwordx4 v[172:175], v12, s[64:67], s79 offen\n"
"s_xor_b32 s79, s84, 0x310000\n"
"buffer_load_dwordx4 v[232:235], v12, s[64:67], s79 offen\n"
"s_xor_b32 s79, s84, 0x318000\n"
"buffer_load_dwordx4 v[236:239], v12, s[64:67], s79 offen\n"
"s_waitcnt vmcnt(40)\n"
"ds_write_b128 v13, a[184:187] offset:0\n"
"ds_write_b128 v13, a[188:191] offset:1088\n"
"ds_write_b128 v13, a[248:251] offset:2176\n"
"ds_write_b128 v13, a[252:255] offset:3264\n"
"s_waitcnt lgkmcnt(0)\n"
"s_barrier\n"
"ds_read_b64_tr_b16 a[184:185], v14 offset:0\n"
"ds_read_b64_tr_b16 a[186:187], v14 offset:544\n"
"ds_read_b64_tr_b16 a[188:189], v14 offset:8704\n"
"ds_read_b64_tr_b16 a[190:191], v14 offset:9248\n"
"ds_read_b64_tr_b16 a[248:249], v14 offset:32\n"
"ds_read_b64_tr_b16 a[250:251], v14 offset:576\n"
"ds_read_b64_tr_b16 a[252:253], v14 offset:8736\n"
"ds_read_b64_tr_b16 a[254:255], v14 offset:9280\n"
"s_xor_b32 s79, s84, 0x380000\n"
"buffer_load_dwordx4 v[176:179], v12, s[64:67], s79 offen\n"
"s_xor_b32 s79, s84, 0x388000\n"
"buffer_load_dwordx4 v[180:183], v12, s[64:67], s79 offen\n"
"s_xor_b32 s79, s84, 0x390000\n"
"buffer_load_dwordx4 v[240:243], v12, s[64:67], s79 offen\n"
"s_xor_b32 s79, s84, 0x398000\n"
"buffer_load_dwordx4 v[244:247], v12, s[64:67], s79 offen\n"
"s_waitcnt vmcnt(40)\n"
"ds_write_b128 v13, a[160:163] offset:17408\n"
"ds_write_b128 v13, a[164:167] offset:18496\n"
"ds_write_b128 v13, a[224:227] offset:19584\n"
"ds_write_b128 v13, a[228:231] offset:20672\n"
"s_waitcnt lgkmcnt(0)\n"
"s_barrier\n"
"ds_read_b64_tr_b16 a[160:161], v14 offset:17408\n"
"ds_read_b64_tr_b16 a[162:163], v14 offset:17952\n"
"ds_read_b64_tr_b16 a[164:165], v14 offset:26112\n"
"ds_read_b64_tr_b16 a[166:167], v14 offset:26656\n"
"ds_read_b64_tr_b16 a[224:225], v14 offset:17440\n"
"ds_read_b64_tr_b16 a[226:227], v14 offset:17984\n"
"ds_read_b64_tr_b16 a[228:229], v14 offset:26144\n"
"ds_read_b64_tr_b16 a[230:231], v14 offset:26688\n"
"s_waitcnt vmcnt(36)\n"
"ds_write_b128 v13, a[168:171] offset:0\n"
"ds_write_b128 v13, a[172:175] offset:1088\n"
"ds_write_b128 v13, a[232:235] offset:2176\n"
"ds_write_b128 v13, a[236:239] offset:3264\n"
"s_waitcnt lgkmcnt(0)\n"
"s_barrier\n"
"ds_read_b64_tr_b16 a[168:169], v14 offset:0\n"
"ds_read_b64_tr_b16 a[170:171], v14 offset:544\n"
"ds_read_b64_tr_b16 a[172:173], v14 offset:8704\n"
"ds_read_b64_tr_b16 a[174:175], v14 offset:9248\n"
"ds_read_b64_tr_b16 a[232:233], v14 offset:32\n"
"ds_read_b64_tr_b16 a[234:235], v14 offset:576\n"
"ds_read_b64_tr_b16 a[236:237], v14 offset:8736\n"
"ds_read_b64_tr_b16 a[238:239], v14 offset:9280\n"
"s_waitcnt vmcnt(32)\n"
"ds_write_b128 v13, a[176:179] offset:17408\n"
"ds_write_b128 v13, a[180:183] offset:18496\n"
"ds_write_b128 v13, a[240:243] offset:19584\n"
"ds_write_b128 v13, a[244:247] offset:20672\n"
"s_waitcnt lgkmcnt(0)\n"
"s_barrier\n"
"ds_read_b64_tr_b16 a[176:177], v14 offset:17408\n"
"ds_read_b64_tr_b16 a[178:179], v14 offset:17952\n"
"ds_read_b64_tr_b16 a[180:181], v14 offset:26112\n"
"ds_read_b64_tr_b16 a[182:183], v14 offset:26656\n"
"ds_read_b64_tr_b16 a[240:241], v14 offset:17440\n"
"ds_read_b64_tr_b16 a[242:243], v14 offset:17984\n"
"ds_read_b64_tr_b16 a[244:245], v14 offset:26144\n"
"ds_read_b64_tr_b16 a[246:247], v14 offset:26688\n"
"s_waitcnt vmcnt(28)\n"
"ds_write_b128 v13, v[152:155] offset:0\n"
"ds_write_b128 v13, v[156:159] offset:1088\n"
"ds_write_b128 v13, v[216:219] offset:2176\n"
"ds_write_b128 v13, v[220:223] offset:3264\n"
"s_waitcnt lgkmcnt(0)\n"
"s_barrier\n"
"ds_read_b64_tr_b16 v[152:153], v14 offset:0\n"
"ds_read_b64_tr_b16 v[154:155], v14 offset:544\n"
"ds_read_b64_tr_b16 v[156:157], v14 offset:8704\n"
"ds_read_b64_tr_b16 v[158:159], v14 offset:9248\n"
"ds_read_b64_tr_b16 v[216:217], v14 offset:32\n"
"ds_read_b64_tr_b16 v[218:219], v14 offset:576\n"
"ds_read_b64_tr_b16 v[220:221], v14 offset:8736\n"
"ds_read_b64_tr_b16 v[222:223], v14 offset:9280\n"
"s_waitcnt vmcnt(24)\n"
"ds_write_b128 v13, v[32:35] offset:17408\n"
"ds_write_b128 v13, v[36:39] offset:18496\n"
"ds_write_b128 v13, v[40:43] offset:19584\n"
"ds_write_b128 v13, v[44:47] offset:20672\n"
"s_waitcnt lgkmcnt(0)\n"
"s_barrier\n"
"ds_read_b64_tr_b16 v[32:33], v14 offset:17408\n"
"ds_read_b64_tr_b16 v[34:35], v14 offset:17952\n"
"ds_read_b64_tr_b16 v[36:37], v14 offset:26112\n"
"ds_read_b64_tr_b16 v[38:39], v14 offset:26656\n"
"ds_read_b64_tr_b16 v[40:41], v14 offset:17440\n"
"ds_read_b64_tr_b16 v[42:43], v14 offset:17984\n"
"ds_read_b64_tr_b16 v[44:45], v14 offset:26144\n"
"ds_read_b64_tr_b16 v[46:47], v14 offset:26688\n"
"s_waitcnt vmcnt(20)\n"
"ds_write_b128 v13, v[48:51] offset:0\n"
"ds_write_b128 v13, v[52:55] offset:1088\n"
"ds_write_b128 v13, v[56:59] offset:2176\n"
"ds_write_b128 v13, v[60:63] offset:3264\n"
"s_waitcnt lgkmcnt(0)\n"
"s_barrier\n"
"ds_read_b64_tr_b16 v[48:49], v14 offset:0\n"
"ds_read_b64_tr_b16 v[50:51], v14 offset:544\n"
"ds_read_b64_tr_b16 v[52:53], v14 offset:8704\n"
"ds_read_b64_tr_b16 v[54:55], v14 offset:9248\n"
"ds_read_b64_tr_b16 v[56:57], v14 offset:32\n"
"ds_read_b64_tr_b16 v[58:59], v14 offset:576\n"
"ds_read_b64_tr_b16 v[60:61], v14 offset:8736\n"
"ds_read_b64_tr_b16 v[62:63], v14 offset:9280\n"
"s_waitcnt vmcnt(16)\n"
"ds_write_b128 v13, v[144:147] offset:17408\n"
"ds_write_b128 v13, v[148:151] offset:18496\n"
"ds_write_b128 v13, v[208:211] offset:19584\n"
"ds_write_b128 v13, v[212:215] offset:20672\n"
"s_waitcnt lgkmcnt(0)\n"
"s_barrier\n"
"ds_read_b64_tr_b16 v[144:145], v14 offset:17408\n"
"ds_read_b64_tr_b16 v[146:147], v14 offset:17952\n"
"ds_read_b64_tr_b16 v[148:149], v14 offset:26112\n"
"ds_read_b64_tr_b16 v[150:151], v14 offset:26656\n"
"ds_read_b64_tr_b16 v[208:209], v14 offset:17440\n"
"ds_read_b64_tr_b16 v[210:211], v14 offset:17984\n"
"ds_read_b64_tr_b16 v[212:213], v14 offset:26144\n"
"ds_read_b64_tr_b16 v[214:215], v14 offset:26688\n"
"s_waitcnt vmcnt(12)\n"
"ds_write_b128 v13, v[184:187] offset:0\n"
"ds_write_b128 v13, v[188:191] offset:1088\n"
"ds_write_b128 v13, v[248:251] offset:2176\n"
"ds_write_b128 v13, v[252:255] offset:3264\n"
"s_waitcnt lgkmcnt(0)\n"
"s_barrier\n"
"ds_read_b64_tr_b16 v[184:185], v14 offset:0\n"
"ds_read_b64_tr_b16 v[186:187], v14 offset:544\n"
"ds_read_b64_tr_b16 v[188:189], v14 offset:8704\n"
"ds_read_b64_tr_b16 v[190:191], v14 offset:9248\n"
"ds_read_b64_tr_b16 v[248:249], v14 offset:32\n"
"ds_read_b64_tr_b16 v[250:251], v14 offset:576\n"
"ds_read_b64_tr_b16 v[252:253], v14 offset:8736\n"
"ds_read_b64_tr_b16 v[254:255], v14 offset:9280\n"
"s_waitcnt vmcnt(8)\n"
"ds_write_b128 v13, v[160:163] offset:17408\n"
"ds_write_b128 v13, v[164:167] offset:18496\n"
"ds_write_b128 v13, v[224:227] offset:19584\n"
"ds_write_b128 v13, v[228:231] offset:20672\n"
"s_waitcnt lgkmcnt(0)\n"
"s_barrier\n"
"ds_read_b64_tr_b16 v[160:161], v14 offset:17408\n"
"ds_read_b64_tr_b16 v[162:163], v14 offset:17952\n"
"ds_read_b64_tr_b16 v[164:165], v14 offset:26112\n"
"ds_read_b64_tr_b16 v[166:167], v14 offset:26656\n"
"ds_read_b64_tr_b16 v[224:225], v14 offset:17440\n"
"ds_read_b64_tr_b16 v[226:227], v14 offset:17984\n"
"ds_read_b64_tr_b16 v[228:229], v14 offset:26144\n"
"ds_read_b64_tr_b16 v[230:231], v14 offset:26688\n"
"s_waitcnt vmcnt(4)\n"
"ds_write_b128 v13, v[168:171] offset:0\n"
"ds_write_b128 v13, v[172:175] offset:1088\n"
"ds_write_b128 v13, v[232:235] offset:2176\n"
"ds_write_b128 v13, v[236:239] offset:3264\n"
"s_waitcnt lgkmcnt(0)\n"
"s_barrier\n"
"s_mov_b32 s79, 0x1b0200\n"
"buffer_load_dwordx2 v[104:105], off, s[52:55], s79 sc1\n"
"s_sub_u32 s64, s34, 0x1a0000\n"
"s_lshl_b32 s64, s64, 5\n"
"s_add_u32 s64, s64, 0x1b2000\n"
"buffer_load_dwordx2 v[106:107], off, s[52:55], s64 sc1\n"
"ds_read_b64_tr_b16 v[168:169], v14 offset:0\n"
"ds_read_b64_tr_b16 v[170:171], v14 offset:544\n"
"ds_read_b64_tr_b16 v[172:173], v14 offset:8704\n"
"ds_read_b64_tr_b16 v[174:175], v14 offset:9248\n"
"ds_read_b64_tr_b16 v[232:233], v14 offset:32\n"
"ds_read_b64_tr_b16 v[234:235], v14 offset:576\n"
"ds_read_b64_tr_b16 v[236:237], v14 offset:8736\n"
"ds_read_b64_tr_b16 v[238:239], v14 offset:9280\n"
"s_waitcnt vmcnt(2)\n"
"ds_write_b128 v13, v[176:179] offset:17408\n"
"ds_write_b128 v13, v[180:183] offset:18496\n"
"ds_write_b128 v13, v[240:243] offset:19584\n"
"ds_write_b128 v13, v[244:247] offset:20672\n"
"s_waitcnt lgkmcnt(0)\n"
"s_barrier\n"
"ds_read_b64_tr_b16 v[176:177], v14 offset:17408\n"
"ds_read_b64_tr_b16 v[178:179], v14 offset:17952\n"
"ds_read_b64_tr_b16 v[180:181], v14 offset:26112\n"
"ds_read_b64_tr_b16 v[182:183], v14 offset:26656\n"
"ds_read_b64_tr_b16 v[240:241], v14 offset:17440\n"
"ds_read_b64_tr_b16 v[242:243], v14 offset:17984\n"
"ds_read_b64_tr_b16 v[244:245], v14 offset:26144\n"
"ds_read_b64_tr_b16 v[246:247], v14 offset:26688\n"
"s_waitcnt lgkmcnt(0)\n"
"ds_write_b128 v11, v[32:35] offset:8192\n"
"ds_write_b128 v11, v[36:39] offset:9216\n"
"ds_write_b128 v11, v[40:43] offset:10240\n"
"ds_write_b128 v11, v[44:47] offset:11264\n"
"ds_write_b128 v11, v[48:51] offset:12288\n"
"ds_write_b128 v11, v[52:55] offset:13312\n"
"ds_write_b128 v11, v[56:59] offset:14336\n"
"ds_write_b128 v11, v[60:63] offset:15360\n"
"s_branch L_sudone_%=\n"
"L_su2_%=:\n"
"s_xor_b32 s79, s84, 0x0\n"
"buffer_load_dwordx4 a[16:19], v12, s[56:59], s79 offen\n"
"s_xor_b32 s79, s84, 0x8000\n"
"buffer_load_dwordx4 a[20:23], v12, s[56:59], s79 offen\n"
"s_xor_b32 s79, s84, 0x10000\n"
"buffer_load_dwordx4 a[80:83], v12, s[56:59], s79 offen\n"
"s_xor_b32 s79, s84, 0x18000\n"
"buffer_load_dwordx4 a[84:87], v12, s[56:59], s79 offen\n"
"s_xor_b32 s79, s84, 0x80000\n"
"buffer_load_dwordx4 a[24:27], v12, s[56:59], s79 offen\n"
"s_xor_b32 s79, s84, 0x88000\n"
"buffer_load_dwordx4 a[28:31], v12, s[56:59], s79 offen\n"
"s_xor_b32 s79, s84, 0x90000\n"
"buffer_load_dwordx4 a[88:91], v12, s[56:59], s79 offen\n"
"s_xor_b32 s79, s84, 0x98000\n"
"buffer_load_dwordx4 a[92:95], v12, s[56:59], s79 offen\n"
"s_xor_b32 s79, s84, 0x100000\n"
"buffer_load_dwordx4 a[0:3], v12, s[56:59], s79 offen\n"
"s_xor_b32 s79, s84, 0x108000\n"
"buffer_load_dwordx4 a[4:7], v12, s[56:59], s79 offen\n"
"s_xor_b32 s79, s84, 0x110000\n"
"buffer_load_dwordx4 a[64:67], v12, s[56:59], s79 offen\n"
"s_xor_b32 s79, s84, 0x118000\n"
"buffer_load_dwordx4 a[68:71], v12, s[56:59], s79 offen\n"
"s_xor_b32 s79, s84, 0x180000\n"
"buffer_load_dwordx4 a[8:11], v12, s[56:59], s79 offen\n"
"s_xor_b32 s79, s84, 0x188000\n"
"buffer_load_dwordx4 a[12:15], v12, s[56:59], s79 offen\n"
"s_xor_b32 s79, s84, 0x190000\n"
"buffer_load_dwordx4 a[72:75], v12, s[56:59], s79 offen\n"
"s_xor_b32 s79, s84, 0x198000\n"
"buffer_load_dwordx4 a[76:79], v12, s[56:59], s79 offen\n"
"s_xor_b32 s79, s84, 0x200000\n"
"buffer_load_dwordx4 a[48:51], v12, s[56:59], s79 offen\n"
"s_xor_b32 s79, s84, 0x208000\n"
"buffer_load_dwordx4 a[52:55], v12, s[56:59], s79 offen\n"
"s_xor_b32 s79, s84, 0x210000\n"
"buffer_load_dwordx4 a[112:115], v12, s[56:59], s79 offen\n"
"s_xor_b32 s79, s84, 0x218000\n"
"buffer_load_dwordx4 a[116:119], v12, s[56:59], s79 offen\n"
"s_xor_b32 s79, s84, 0x280000\n"
"buffer_load_dwordx4 a[56:59], v12, s[56:59], s79 offen\n"
"s_xor_b32 s79, s84, 0x288000\n"
"buffer_load_dwordx4 a[60:63], v12, s[56:59], s79 offen\n"
"s_xor_b32 s79, s84, 0x290000\n"
"buffer_load_dwordx4 a[120:123], v12, s[56:59], s79 offen\n"
"s_xor_b32 s79, s84, 0x298000\n"
"buffer_load_dwordx4 a[124:127], v12, s[56:59], s79 offen\n"
"s_xor_b32 s79, s84, 0x300000\n"
"buffer_load_dwordx4 a[32:35], v12, s[56:59], s79 offen\n"
"s_xor_b32 s79, s84, 0x308000\n"
"buffer_load_dwordx4 a[36:39], v12, s[56:59], s79 offen\n"
"s_xor_b32 s79, s84, 0x310000\n"
"buffer_load_dwordx4 a[96:99], v12, s[56:59], s79 offen\n"
"s_xor_b32 s79, s84, 0x318000\n"
"buffer_load_dwordx4 a[100:103], v12, s[56:59], s79 offen\n"
"s_xor_b32 s79, s84, 0x380000\n"
"buffer_load_dwordx4 a[40:43], v12, s[56:59], s79 offen\n"
"s_xor_b32 s79, s84, 0x388000\n"
"buffer_load_dwordx4 a[44:47], v12, s[56:59], s79 offen\n"
"s_xor_b32 s79, s84, 0x390000\n"
"buffer_load_dwordx4 a[104:107], v12, s[56:59], s79 offen\n"
"s_xor_b32 s79, s84, 0x398000\n"
"buffer_load_dwordx4 a[108:111], v12, s[56:59], s79 offen\n"
"s_xor_b32 s79, s84, 0x0\n"
"buffer_load_dwordx4 a[144:147], v12, s[60:63], s79 offen\n"
"s_xor_b32 s79, s84, 0x8000\n"
"buffer_load_dwordx4 a[148:151], v12, s[60:63], s79 offen\n"
"s_xor_b32 s79, s84, 0x10000\n"
"buffer_load_dwordx4 a[208:211], v12, s[60:63], s79 offen\n"
"s_xor_b32 s79, s84, 0x18000\n"
"buffer_load_dwordx4 a[212:215], v12, s[60:63], s79 offen\n"
"s_xor_b32 s79, s84, 0x80000\n"
"buffer_load_dwordx4 a[152:155], v12, s[60:63], s79 offen\n"
"s_xor_b32 s79, s84, 0x88000\n"
"buffer_load_dwordx4 a[156:159], v12, s[60:63], s79 offen\n"
"s_xor_b32 s79, s84, 0x90000\n"
"buffer_load_dwordx4 a[216:219], v12, s[60:63], s79 offen\n"
"s_xor_b32 s79, s84, 0x98000\n"
"buffer_load_dwordx4 a[220:223], v12, s[60:63], s79 offen\n"
"s_waitcnt vmcnt(40)\n"
"ds_write_b128 v11, v[32:35] offset:0\n"
"ds_write_b128 v11, v[36:39] offset:1024\n"
"ds_write_b128 v11, v[40:43] offset:2048\n"
"ds_write_b128 v11, v[44:47] offset:3072\n"
"ds_write_b128 v11, v[48:51] offset:4096\n"
"ds_write_b128 v11, v[52:55] offset:5120\n"
"ds_write_b128 v11, v[56:59] offset:6144\n"
"ds_write_b128 v11, v[60:63] offset:7168\n"
"ds_write_b128 v11, v[64:67] offset:8192\n"
"ds_write_b128 v11, v[68:71] offset:9216\n"
"ds_write_b128 v11, v[72:75] offset:10240\n"
"ds_write_b128 v11, v[76:79] offset:11264\n"
"ds_write_b128 v11, v[80:83] offset:12288\n"
"ds_write_b128 v11, v[84:87] offset:13312\n"
"ds_write_b128 v11, v[88:91] offset:14336\n"
"ds_write_b128 v11, v[92:95] offset:15360\n"
"ds_read_b64_tr_b16 v[128:129], v16 offset:0\n"
"ds_read_b64_tr_b16 v[130:131], v16 offset:256\n"
"ds_read_b64_tr_b16 v[132:133], v16 offset:4096\n"
"ds_read_b64_tr_b16 v[134:135], v16 offset:4352\n"
"ds_read_b64_tr_b16 v[136:137], v16 offset:32\n"
"ds_read_b64_tr_b16 v[138:139], v16 offset:288\n"
"ds_read_b64_tr_b16 v[140:141], v16 offset:4128\n"
"ds_read_b64_tr_b16 v[142:143], v16 offset:4384\n"
"ds_read_b64_tr_b16 v[32:33], v16 offset:64\n"
"ds_read_b64_tr_b16 v[34:35], v16 offset:320\n"
"ds_read_b64_tr_b16 v[36:37], v16 offset:4160\n"
"ds_read_b64_tr_b16 v[38:39], v16 offset:4416\n"
"ds_read_b64_tr_b16 v[40:41], v16 offset:96\n"
"ds_read_b64_tr_b16 v[42:43], v16 offset:352\n"
"ds_read_b64_tr_b16 v[44:45], v16 offset:4192\n"
"ds_read_b64_tr_b16 v[46:47], v16 offset:4448\n"
"ds_read_b64_tr_b16 v[192:193], v16 offset:8192\n"
"ds_read_b64_tr_b16 v[194:195], v16 offset:8448\n"
"ds_read_b64_tr_b16 v[196:197], v16 offset:12288\n"
"ds_read_b64_tr_b16 v[198:199], v16 offset:12544\n"
"ds_read_b64_tr_b16 v[200:201], v16 offset:8224\n"
"ds_read_b64_tr_b16 v[202:203], v16 offset:8480\n"
"ds_read_b64_tr_b16 v[204:205], v16 offset:12320\n"
"ds_read_b64_tr_b16 v[206:207], v16 offset:12576\n"
"ds_read_b64_tr_b16 v[48:49], v16 offset:8256\n"
"ds_read_b64_tr_b16 v[50:51], v16 offset:8512\n"
"ds_read_b64_tr_b16 v[52:53], v16 offset:12352\n"
"ds_read_b64_tr_b16 v[54:55], v16 offset:12608\n"
"ds_read_b64_tr_b16 v[56:57], v16 offset:8288\n"
"ds_read_b64_tr_b16 v[58:59], v16 offset:8544\n"
"ds_read_b64_tr_b16 v[60:61], v16 offset:12384\n"
"ds_read_b64_tr_b16 v[62:63], v16 offset:12640\n"
"s_waitcnt lgkmcnt(0)\n"
"ds_write_b128 v11, v[32:35] offset:0\n"
"ds_write_b128 v11, v[36:39] offset:1024\n"
"ds_write_b128 v11, v[40:43] offset:2048\n"
"ds_write_b128 v11, v[44:47] offset:3072\n"
"ds_write_b128 v11, v[48:51] offset:4096\n"
"ds_write_b128 v11, v[52:55] offset:5120\n"
"ds_write_b128 v11, v[56:59] offset:6144\n"
"ds_write_b128 v11, v[60:63] offset:7168\n"
"s_xor_b32 s79, s84, 0x100000\n"
"buffer_load_dwordx4 a[128:131], v12, s[60:63], s79 offen\n"
"s_xor_b32 s79, s84, 0x108000\n"
"buffer_load_dwordx4 a[132:135], v12, s[60:63], s79 offen\n"
"s_xor_b32 s79, s84, 0x110000\n"
"buffer_load_dwordx4 a[192:195], v12, s[60:63], s79 offen\n"
"s_xor_b32 s79, s84, 0x118000\n"
"buffer_load_dwordx4 a[196:199], v12, s[60:63], s79 offen\n"
"s_waitcnt vmcnt(40)\n"
"ds_write_b128 v13, a[16:19] offset:0\n"
"ds_write_b128 v13, a[20:23] offset:1088\n"
"ds_write_b128 v13, a[80:83] offset:2176\n"
"ds_write_b128 v13, a[84:87] offset:3264\n"
"s_waitcnt lgkmcnt(0)\n"
"s_barrier\n"
"ds_read_b64_tr_b16 a[16:17], v14 offset:0\n"
"ds_read_b64_tr_b16 a[18:19], v14 offset:544\n"
"ds_read_b64_tr_b16 a[20:21], v14 offset:8704\n"
"ds_read_b64_tr_b16 a[22:23], v14 offset:9248\n"
"ds_read_b64_tr_b16 a[80:81], v14 offset:32\n"
"ds_read_b64_tr_b16 a[82:83], v14 offset:576\n"
"ds_read_b64_tr_b16 a[84:85], v14 offset:8736\n"
"ds_read_b64_tr_b16 a[86:87], v14 offset:9280\n"
"s_xor_b32 s79, s84, 0x180000\n"
"buffer_load_dwordx4 a[136:139], v12, s[60:63], s79 offen\n"
"s_xor_b32 s79, s84, 0x188000\n"
"buffer_load_dwordx4 a[140:143], v12, s[60:63], s79 offen\n"
"s_xor_b32 s79, s84, 0x190000\n"
"buffer_load_dwordx4 a[200:203], v12, s[60:63], s79 offen\n"
"s_xor_b32 s79, s84, 0x198000\n"
"buffer_load_dwordx4 a[204:207], v12, s[60:63], s79 offen\n"
"s_waitcnt vmcnt(40)\n"
"ds_write_b128 v13, a[24:27] offset:17408\n"
"ds_write_b128 v13, a[28:31] offset:18496\n"
"ds_write_b128 v13, a[88:91] offset:19584\n"
"ds_write_b128 v13, a[92:95] offset:20672\n"
"s_waitcnt lgkmcnt(0)\n"
"s_barrier\n"
"ds_read_b64_tr_b16 a[24:25], v14 offset:17408\n"
"ds_read_b64_tr_b16 a[26:27], v14 offset:17952\n"
"ds_read_b64_tr_b16 a[28:29], v14 offset:26112\n"
"ds_read_b64_tr_b16 a[30:31], v14 offset:26656\n"
"ds_read_b64_tr_b16 a[88:89], v14 offset:17440\n"
"ds_read_b64_tr_b16 a[90:91], v14 offset:17984\n"
"ds_read_b64_tr_b16 a[92:93], v14 offset:26144\n"
"ds_read_b64_tr_b16 a[94:95], v14 offset:26688\n"
"s_xor_b32 s79, s84, 0x200000\n"
"buffer_load_dwordx4 a[176:179], v12, s[60:63], s79 offen\n"
"s_xor_b32 s79, s84, 0x208000\n"
"buffer_load_dwordx4 a[180:183], v12, s[60:63], s79 offen\n"
"s_xor_b32 s79, s84, 0x210000\n"
"buffer_load_dwordx4 a[240:243], v12, s[60:63], s79 offen\n"
"s_xor_b32 s79, s84, 0x218000\n"
"buffer_load_dwordx4 a[244:247], v12, s[60:63], s79 offen\n"
"s_waitcnt vmcnt(40)\n"
"ds_write_b128 v13, a[0:3] offset:0\n"
"ds_write_b128 v13, a[4:7] offset:1088\n"
"ds_write_b128 v13, a[64:67] offset:2176\n"
"ds_write_b128 v13, a[68:71] offset:3264\n"
"s_waitcnt lgkmcnt(0)\n"
"s_barrier\n"
"ds_read_b64_tr_b16 a[0:1], v14 offset:0\n"
"ds_read_b64_tr_b16 a[2:3], v14 offset:544\n"
"ds_read_b64_tr_b16 a[4:5], v14 offset:8704\n"
"ds_read_b64_tr_b16 a[6:7], v14 offset:9248\n"
"ds_read_b64_tr_b16 a[64:65], v14 offset:32\n"
"ds_read_b64_tr_b16 a[66:67], v14 offset:576\n"
"ds_read_b64_tr_b16 a[68:69], v14 offset:8736\n"
"ds_read_b64_tr_b16 a[70:71], v14 offset:9280\n"
"s_xor_b32 s79, s84, 0x280000\n"
"buffer_load_dwordx4 a[184:187], v12, s[60:63], s79 offen\n"
"s_xor_b32 s79, s84, 0x288000\n"
"buffer_load_dwordx4 a[188:191], v12, s[60:63], s79 offen\n"
"s_xor_b32 s79, s84, 0x290000\n"
"buffer_load_dwordx4 a[248:251], v12, s[60:63], s79 offen\n"
"s_xor_b32 s79, s84, 0x298000\n"
"buffer_load_dwordx4 a[252:255], v12, s[60:63], s79 offen\n"
"s_waitcnt vmcnt(40)\n"
"ds_write_b128 v13, a[8:11] offset:17408\n"
"ds_write_b128 v13, a[12:15] offset:18496\n"
"ds_write_b128 v13, a[72:75] offset:19584\n"
"ds_write_b128 v13, a[76:79] offset:20672\n"
"s_waitcnt lgkmcnt(0)\n"
"s_barrier\n"
"ds_read_b64_tr_b16 a[8:9], v14 offset:17408\n"
"ds_read_b64_tr_b16 a[10:11], v14 offset:17952\n"
"ds_read_b64_tr_b16 a[12:13], v14 offset:26112\n"
"ds_read_b64_tr_b16 a[14:15], v14 offset:26656\n"
"ds_read_b64_tr_b16 a[72:73], v14 offset:17440\n"
"ds_read_b64_tr_b16 a[74:75], v14 offset:17984\n"
"ds_read_b64_tr_b16 a[76:77], v14 offset:26144\n"
"ds_read_b64_tr_b16 a[78:79], v14 offset:26688\n"
"s_xor_b32 s79, s84, 0x300000\n"
"buffer_load_dwordx4 a[160:163], v12, s[60:63], s79 offen\n"
"s_xor_b32 s79, s84, 0x308000\n"
"buffer_load_dwordx4 a[164:167], v12, s[60:63], s79 offen\n"
"s_xor_b32 s79, s84, 0x310000\n"
"buffer_load_dwordx4 a[224:227], v12, s[60:63], s79 offen\n"
"s_xor_b32 s79, s84, 0x318000\n"
"buffer_load_dwordx4 a[228:231], v12, s[60:63], s79 offen\n"
"s_waitcnt vmcnt(40)\n"
"ds_write_b128 v13, a[48:51] offset:0\n"
"ds_write_b128 v13, a[52:55] offset:1088\n"
"ds_write_b128 v13, a[112:115] offset:2176\n"
"ds_write_b128 v13, a[116:119] offset:3264\n"
"s_waitcnt lgkmcnt(0)\n"
"s_barrier\n"
"ds_read_b64_tr_b16 a[48:49], v14 offset:0\n"
"ds_read_b64_tr_b16 a[50:51], v14 offset:544\n"
"ds_read_b64_tr_b16 a[52:53], v14 offset:8704\n"
"ds_read_b64_tr_b16 a[54:55], v14 offset:9248\n"
"ds_read_b64_tr_b16 a[112:113], v14 offset:32\n"
"ds_read_b64_tr_b16 a[114:115], v14 offset:576\n"
"ds_read_b64_tr_b16 a[116:117], v14 offset:8736\n"
"ds_read_b64_tr_b16 a[118:119], v14 offset:9280\n"
"s_xor_b32 s79, s84, 0x380000\n"
"buffer_load_dwordx4 a[168:171], v12, s[60:63], s79 offen\n"
"s_xor_b32 s79, s84, 0x388000\n"
"buffer_load_dwordx4 a[172:175], v12, s[60:63], s79 offen\n"
"s_xor_b32 s79, s84, 0x390000\n"
"buffer_load_dwordx4 a[232:235], v12, s[60:63], s79 offen\n"
"s_xor_b32 s79, s84, 0x398000\n"
"buffer_load_dwordx4 a[236:239], v12, s[60:63], s79 offen\n"
"s_waitcnt vmcnt(40)\n"
"ds_write_b128 v13, a[56:59] offset:17408\n"
"ds_write_b128 v13, a[60:63] offset:18496\n"
"ds_write_b128 v13, a[120:123] offset:19584\n"
"ds_write_b128 v13, a[124:127] offset:20672\n"
"s_waitcnt lgkmcnt(0)\n"
"s_barrier\n"
"ds_read_b64_tr_b16 a[56:57], v14 offset:17408\n"
"ds_read_b64_tr_b16 a[58:59], v14 offset:17952\n"
"ds_read_b64_tr_b16 a[60:61], v14 offset:26112\n"
"ds_read_b64_tr_b16 a[62:63], v14 offset:26656\n"
"ds_read_b64_tr_b16 a[120:121], v14 offset:17440\n"
"ds_read_b64_tr_b16 a[122:123], v14 offset:17984\n"
"ds_read_b64_tr_b16 a[124:125], v14 offset:26144\n"
"ds_read_b64_tr_b16 a[126:127], v14 offset:26688\n"
"s_xor_b32 s79, s84, 0x0\n"
"buffer_load_dwordx4 v[144:147], v12, s[64:67], s79 offen\n"
"s_xor_b32 s79, s84, 0x8000\n"
"buffer_load_dwordx4 v[148:151], v12, s[64:67], s79 offen\n"
"s_xor_b32 s79, s84, 0x10000\n"
"buffer_load_dwordx4 v[208:211], v12, s[64:67], s79 offen\n"
"s_xor_b32 s79, s84, 0x18000\n"
"buffer_load_dwordx4 v[212:215], v12, s[64:67], s79 offen\n"
"s_waitcnt vmcnt(40)\n"
"ds_write_b128 v13, a[32:35] offset:0\n"
"ds_write_b128 v13, a[36:39] offset:1088\n"
"ds_write_b128 v13, a[96:99] offset:2176\n"
"ds_write_b128 v13, a[100:103] offset:3264\n"
"s_waitcnt lgkmcnt(0)\n"
"s_barrier\n"
"ds_read_b64_tr_b16 a[32:33], v14 offset:0\n"
"ds_read_b64_tr_b16 a[34:35], v14 offset:544\n"
"ds_read_b64_tr_b16 a[36:37], v14 offset:8704\n"
"ds_read_b64_tr_b16 a[38:39], v14 offset:9248\n"
"ds_read_b64_tr_b16 a[96:97], v14 offset:32\n"
"ds_read_b64_tr_b16 a[98:99], v14 offset:576\n"
"ds_read_b64_tr_b16 a[100:101], v14 offset:8736\n"
"ds_read_b64_tr_b16 a[102:103], v14 offset:9280\n"
"s_xor_b32 s79, s84, 0x80000\n"
"buffer_load_dwordx4 v[152:155], v12, s[64:67], s79 offen\n"
"s_xor_b32 s79, s84, 0x88000\n"
"buffer_load_dwordx4 v[156:159], v12, s[64:67], s79 offen\n"
"s_xor_b32 s79, s84, 0x90000\n"
"buffer_load_dwordx4 v[216:219], v12, s[64:67], s79 offen\n"
"s_xor_b32 s79, s84, 0x98000\n"
"buffer_load_dwordx4 v[220:223], v12, s[64:67], s79 offen\n"
"s_waitcnt vmcnt(40)\n"
"ds_write_b128 v13, a[40:43] offset:17408\n"
"ds_write_b128 v13, a[44:47] offset:18496\n"
"ds_write_b128 v13, a[104:107] offset:19584\n"
"ds_write_b128 v13, a[108:111] offset:20672\n"
"s_waitcnt lgkmcnt(0)\n"
"s_barrier\n"
"ds_read_b64_tr_b16 a[40:41], v14 offset:17408\n"
"ds_read_b64_tr_b16 a[42:43], v14 offset:17952\n"
"ds_read_b64_tr_b16 a[44:45], v14 offset:26112\n"
"ds_read_b64_tr_b16 a[46:47], v14 offset:26656\n"
"ds_read_b64_tr_b16 a[104:105], v14 offset:17440\n"
"ds_read_b64_tr_b16 a[106:107], v14 offset:17984\n"
"ds_read_b64_tr_b16 a[108:109], v14 offset:26144\n"
"ds_read_b64_tr_b16 a[110:111], v14 offset:26688\n"
"s_xor_b32 s79, s84, 0x100000\n"
"buffer_load_dwordx4 v[32:35], v12, s[64:67], s79 offen\n"
"s_xor_b32 s79, s84, 0x108000\n"
"buffer_load_dwordx4 v[36:39], v12, s[64:67], s79 offen\n"
"s_xor_b32 s79, s84, 0x110000\n"
"buffer_load_dwordx4 v[40:43], v12, s[64:67], s79 offen\n"
"s_xor_b32 s79, s84, 0x118000\n"
"buffer_load_dwordx4 v[44:47], v12, s[64:67], s79 offen\n"
"s_waitcnt vmcnt(40)\n"
"ds_write_b128 v13, a[144:147] offset:0\n"
"ds_write_b128 v13, a[148:151] offset:1088\n"
"ds_write_b128 v13, a[208:211] offset:2176\n"
"ds_write_b128 v13, a[212:215] offset:3264\n"
"s_waitcnt lgkmcnt(0)\n"
"s_barrier\n"
"ds_read_b64_tr_b16 a[144:145], v14 offset:0\n"
"ds_read_b64_tr_b16 a[146:147], v14 offset:544\n"
"ds_read_b64_tr_b16 a[148:149], v14 offset:8704\n"
"ds_read_b64_tr_b16 a[150:151], v14 offset:9248\n"
"ds_read_b64_tr_b16 a[208:209], v14 offset:32\n"
"ds_read_b64_tr_b16 a[210:211], v14 offset:576\n"
"ds_read_b64_tr_b16 a[212:213], v14 offset:8736\n"
"ds_read_b64_tr_b16 a[214:215], v14 offset:9280\n"
"s_xor_b32 s79, s84, 0x180000\n"
"buffer_load_dwordx4 v[48:51], v12, s[64:67], s79 offen\n"
"s_xor_b32 s79, s84, 0x188000\n"
"buffer_load_dwordx4 v[52:55], v12, s[64:67], s79 offen\n"
"s_xor_b32 s79, s84, 0x190000\n"
"buffer_load_dwordx4 v[56:59], v12, s[64:67], s79 offen\n"
"s_xor_b32 s79, s84, 0x198000\n"
"buffer_load_dwordx4 v[60:63], v12, s[64:67], s79 offen\n"
"s_waitcnt vmcnt(40)\n"
"ds_write_b128 v13, a[152:155] offset:17408\n"
"ds_write_b128 v13, a[156:159] offset:18496\n"
"ds_write_b128 v13, a[216:219] offset:19584\n"
"ds_write_b128 v13, a[220:223] offset:20672\n"
"s_waitcnt lgkmcnt(0)\n"
"s_barrier\n"
"ds_read_b64_tr_b16 a[152:153], v14 offset:17408\n"
"ds_read_b64_tr_b16 a[154:155], v14 offset:17952\n"
"ds_read_b64_tr_b16 a[156:157], v14 offset:26112\n"
"ds_read_b64_tr_b16 a[158:159], v14 offset:26656\n"
"ds_read_b64_tr_b16 a[216:217], v14 offset:17440\n"
"ds_read_b64_tr_b16 a[218:219], v14 offset:17984\n"
"ds_read_b64_tr_b16 a[220:221], v14 offset:26144\n"
"ds_read_b64_tr_b16 a[222:223], v14 offset:26688\n"
"s_xor_b32 s79, s84, 0x200000\n"
"buffer_load_dwordx4 v[176:179], v12, s[64:67], s79 offen\n"
"s_xor_b32 s79, s84, 0x208000\n"
"buffer_load_dwordx4 v[180:183], v12, s[64:67], s79 offen\n"
"s_xor_b32 s79, s84, 0x210000\n"
"buffer_load_dwordx4 v[240:243], v12, s[64:67], s79 offen\n"
"s_xor_b32 s79, s84, 0x218000\n"
"buffer_load_dwordx4 v[244:247], v12, s[64:67], s79 offen\n"
"s_waitcnt vmcnt(40)\n"
"ds_write_b128 v13, a[128:131] offset:0\n"
"ds_write_b128 v13, a[132:135] offset:1088\n"
"ds_write_b128 v13, a[192:195] offset:2176\n"
"ds_write_b128 v13, a[196:199] offset:3264\n"
"s_waitcnt lgkmcnt(0)\n"
"s_barrier\n"
"ds_read_b64_tr_b16 a[128:129], v14 offset:0\n"
"ds_read_b64_tr_b16 a[130:131], v14 offset:544\n"
"ds_read_b64_tr_b16 a[132:133], v14 offset:8704\n"
"ds_read_b64_tr_b16 a[134:135], v14 offset:9248\n"
"ds_read_b64_tr_b16 a[192:193], v14 offset:32\n"
"ds_read_b64_tr_b16 a[194:195], v14 offset:576\n"
"ds_read_b64_tr_b16 a[196:197], v14 offset:8736\n"
"ds_read_b64_tr_b16 a[198:199], v14 offset:9280\n"
"s_xor_b32 s79, s84, 0x280000\n"
"buffer_load_dwordx4 v[184:187], v12, s[64:67], s79 offen\n"
"s_xor_b32 s79, s84, 0x288000\n"
"buffer_load_dwordx4 v[188:191], v12, s[64:67], s79 offen\n"
"s_xor_b32 s79, s84, 0x290000\n"
"buffer_load_dwordx4 v[248:251], v12, s[64:67], s79 offen\n"
"s_xor_b32 s79, s84, 0x298000\n"
"buffer_load_dwordx4 v[252:255], v12, s[64:67], s79 offen\n"
"s_waitcnt vmcnt(40)\n"
"ds_write_b128 v13, a[136:139] offset:17408\n"
"ds_write_b128 v13, a[140:143] offset:18496\n"
"ds_write_b128 v13, a[200:203] offset:19584\n"
"ds_write_b128 v13, a[204:207] offset:20672\n"
"s_waitcnt lgkmcnt(0)\n"
"s_barrier\n"
"ds_read_b64_tr_b16 a[136:137], v14 offset:17408\n"
"ds_read_b64_tr_b16 a[138:139], v14 offset:17952\n"
"ds_read_b64_tr_b16 a[140:141], v14 offset:26112\n"
"ds_read_b64_tr_b16 a[142:143], v14 offset:26656\n"
"ds_read_b64_tr_b16 a[200:201], v14 offset:17440\n"
"ds_read_b64_tr_b16 a[202:203], v14 offset:17984\n"
"ds_read_b64_tr_b16 a[204:205], v14 offset:26144\n"
"ds_read_b64_tr_b16 a[206:207], v14 offset:26688\n"
"s_xor_b32 s79, s84, 0x300000\n"
"buffer_load_dwordx4 v[160:163], v12, s[64:67], s79 offen\n"
"s_xor_b32 s79, s84, 0x308000\n"
"buffer_load_dwordx4 v[164:167], v12, s[64:67], s79 offen\n"
"s_xor_b32 s79, s84, 0x310000\n"
"buffer_load_dwordx4 v[224:227], v12, s[64:67], s79 offen\n"
"s_xor_b32 s79, s84, 0x318000\n"
"buffer_load_dwordx4 v[228:231], v12, s[64:67], s79 offen\n"
"s_waitcnt vmcnt(40)\n"
"ds_write_b128 v13, a[176:179] offset:0\n"
"ds_write_b128 v13, a[180:183] offset:1088\n"
"ds_write_b128 v13, a[240:243] offset:2176\n"
"ds_write_b128 v13, a[244:247] offset:3264\n"
"s_waitcnt lgkmcnt(0)\n"
"s_barrier\n"
"ds_read_b64_tr_b16 a[176:177], v14 offset:0\n"
"ds_read_b64_tr_b16 a[178:179], v14 offset:544\n"
"ds_read_b64_tr_b16 a[180:181], v14 offset:8704\n"
"ds_read_b64_tr_b16 a[182:183], v14 offset:9248\n"
"ds_read_b64_tr_b16 a[240:241], v14 offset:32\n"
"ds_read_b64_tr_b16 a[242:243], v14 offset:576\n"
"ds_read_b64_tr_b16 a[244:245], v14 offset:8736\n"
"ds_read_b64_tr_b16 a[246:247], v14 offset:9280\n"
"s_xor_b32 s79, s84, 0x380000\n"
"buffer_load_dwordx4 v[168:171], v12, s[64:67], s79 offen\n"
"s_xor_b32 s79, s84, 0x388000\n"
"buffer_load_dwordx4 v[172:175], v12, s[64:67], s79 offen\n"
"s_xor_b32 s79, s84, 0x390000\n"
"buffer_load_dwordx4 v[232:235], v12, s[64:67], s79 offen\n"
"s_xor_b32 s79, s84, 0x398000\n"
"buffer_load_dwordx4 v[236:239], v12, s[64:67], s79 offen\n"
"s_waitcnt vmcnt(40)\n"
"ds_write_b128 v13, a[184:187] offset:17408\n"
"ds_write_b128 v13, a[188:191] offset:18496\n"
"ds_write_b128 v13, a[248:251] offset:19584\n"
"ds_write_b128 v13, a[252:255] offset:20672\n"
"s_waitcnt lgkmcnt(0)\n"
"s_barrier\n"
"ds_read_b64_tr_b16 a[184:185], v14 offset:17408\n"
"ds_read_b64_tr_b16 a[186:187], v14 offset:17952\n"
"ds_read_b64_tr_b16 a[188:189], v14 offset:26112\n"
"ds_read_b64_tr_b16 a[190:191], v14 offset:26656\n"
"ds_read_b64_tr_b16 a[248:249], v14 offset:17440\n"
"ds_read_b64_tr_b16 a[250:251], v14 offset:17984\n"
"ds_read_b64_tr_b16 a[252:253], v14 offset:26144\n"
"ds_read_b64_tr_b16 a[254:255], v14 offset:26688\n"
"s_waitcnt vmcnt(36)\n"
"ds_write_b128 v13, a[160:163] offset:0\n"
"ds_write_b128 v13, a[164:167] offset:1088\n"
"ds_write_b128 v13, a[224:227] offset:2176\n"
"ds_write_b128 v13, a[228:231] offset:3264\n"
"s_waitcnt lgkmcnt(0)\n"
"s_barrier\n"
"ds_read_b64_tr_b16 a[160:161], v14 offset:0\n"
"ds_read_b64_tr_b16 a[162:163], v14 offset:544\n"
"ds_read_b64_tr_b16 a[164:165], v14 offset:8704\n"
"ds_read_b64_tr_b16 a[166:167], v14 offset:9248\n"
"ds_read_b64_tr_b16 a[224:225], v14 offset:32\n"
"ds_read_b64_tr_b16 a[226:227], v14 offset:576\n"
"ds_read_b64_tr_b16 a[228:229], v14 offset:8736\n"
"ds_read_b64_tr_b16 a[230:231], v14 offset:9280\n"
"s_waitcnt vmcnt(32)\n"
"ds_write_b128 v13, a[168:171] offset:17408\n"
"ds_write_b128 v13, a[172:175] offset:18496\n"
"ds_write_b128 v13, a[232:235] offset:19584\n"
"ds_write_b128 v13, a[236:239] offset:20672\n"
"s_waitcnt lgkmcnt(0)\n"
"s_barrier\n"
"ds_read_b64_tr_b16 a[168:169], v14 offset:17408\n"
"ds_read_b64_tr_b16 a[170:171], v14 offset:17952\n"
"ds_read_b64_tr_b16 a[172:173], v14 offset:26112\n"
"ds_read_b64_tr_b16 a[174:175], v14 offset:26656\n"
"ds_read_b64_tr_b16 a[232:233], v14 offset:17440\n"
"ds_read_b64_tr_b16 a[234:235], v14 offset:17984\n"
"ds_read_b64_tr_b16 a[236:237], v14 offset:26144\n"
"ds_read_b64_tr_b16 a[238:239], v14 offset:26688\n"
"s_waitcnt vmcnt(28)\n"
"ds_write_b128 v13, v[144:147] offset:0\n"
"ds_write_b128 v13, v[148:151] offset:1088\n"
"ds_write_b128 v13, v[208:211] offset:2176\n"
"ds_write_b128 v13, v[212:215] offset:3264\n"
"s_waitcnt lgkmcnt(0)\n"
"s_barrier\n"
"ds_read_b64_tr_b16 v[144:145], v14 offset:0\n"
"ds_read_b64_tr_b16 v[146:147], v14 offset:544\n"
"ds_read_b64_tr_b16 v[148:149], v14 offset:8704\n"
"ds_read_b64_tr_b16 v[150:151], v14 offset:9248\n"
"ds_read_b64_tr_b16 v[208:209], v14 offset:32\n"
"ds_read_b64_tr_b16 v[210:211], v14 offset:576\n"
"ds_read_b64_tr_b16 v[212:213], v14 offset:8736\n"
"ds_read_b64_tr_b16 v[214:215], v14 offset:9280\n"
"s_waitcnt vmcnt(24)\n"
"ds_write_b128 v13, v[152:155] offset:17408\n"
"ds_write_b128 v13, v[156:159] offset:18496\n"
"ds_write_b128 v13, v[216:219] offset:19584\n"
"ds_write_b128 v13, v[220:223] offset:20672\n"
"s_waitcnt lgkmcnt(0)\n"
"s_barrier\n"
"ds_read_b64_tr_b16 v[152:153], v14 offset:17408\n"
"ds_read_b64_tr_b16 v[154:155], v14 offset:17952\n"
"ds_read_b64_tr_b16 v[156:157], v14 offset:26112\n"
"ds_read_b64_tr_b16 v[158:159], v14 offset:26656\n"
"ds_read_b64_tr_b16 v[216:217], v14 offset:17440\n"
"ds_read_b64_tr_b16 v[218:219], v14 offset:17984\n"
"ds_read_b64_tr_b16 v[220:221], v14 offset:26144\n"
"ds_read_b64_tr_b16 v[222:223], v14 offset:26688\n"
"s_waitcnt vmcnt(20)\n"
"ds_write_b128 v13, v[32:35] offset:0\n"
"ds_write_b128 v13, v[36:39] offset:1088\n"
"ds_write_b128 v13, v[40:43] offset:2176\n"
"ds_write_b128 v13, v[44:47] offset:3264\n"
"s_waitcnt lgkmcnt(0)\n"
"s_barrier\n"
"ds_read_b64_tr_b16 v[32:33], v14 offset:0\n"
"ds_read_b64_tr_b16 v[34:35], v14 offset:544\n"
"ds_read_b64_tr_b16 v[36:37], v14 offset:8704\n"
"ds_read_b64_tr_b16 v[38:39], v14 offset:9248\n"
"ds_read_b64_tr_b16 v[40:41], v14 offset:32\n"
"ds_read_b64_tr_b16 v[42:43], v14 offset:576\n"
"ds_read_b64_tr_b16 v[44:45], v14 offset:8736\n"
"ds_read_b64_tr_b16 v[46:47], v14 offset:9280\n"
"s_waitcnt vmcnt(16)\n"
"ds_write_b128 v13, v[48:51] offset:17408\n"
"ds_write_b128 v13, v[52:55] offset:18496\n"
"ds_write_b128 v13, v[56:59] offset:19584\n"
"ds_write_b128 v13, v[60:63] offset:20672\n"
"s_waitcnt lgkmcnt(0)\n"
"s_barrier\n"
"ds_read_b64_tr_b16 v[48:49], v14 offset:17408\n"
"ds_read_b64_tr_b16 v[50:51], v14 offset:17952\n"
"ds_read_b64_tr_b16 v[52:53], v14 offset:26112\n"
"ds_read_b64_tr_b16 v[54:55], v14 offset:26656\n"
"ds_read_b64_tr_b16 v[56:57], v14 offset:17440\n"
"ds_read_b64_tr_b16 v[58:59], v14 offset:17984\n"
"ds_read_b64_tr_b16 v[60:61], v14 offset:26144\n"
"ds_read_b64_tr_b16 v[62:63], v14 offset:26688\n"
"s_waitcnt vmcnt(12)\n"
"ds_write_b128 v13, v[176:179] offset:0\n"
"ds_write_b128 v13, v[180:183] offset:1088\n"
"ds_write_b128 v13, v[240:243] offset:2176\n"
"ds_write_b128 v13, v[244:247] offset:3264\n"
"s_waitcnt lgkmcnt(0)\n"
"s_barrier\n"
"ds_read_b64_tr_b16 v[176:177], v14 offset:0\n"
"ds_read_b64_tr_b16 v[178:179], v14 offset:544\n"
"ds_read_b64_tr_b16 v[180:181], v14 offset:8704\n"
"ds_read_b64_tr_b16 v[182:183], v14 offset:9248\n"
"ds_read_b64_tr_b16 v[240:241], v14 offset:32\n"
"ds_read_b64_tr_b16 v[242:243], v14 offset:576\n"
"ds_read_b64_tr_b16 v[244:245], v14 offset:8736\n"
"ds_read_b64_tr_b16 v[246:247], v14 offset:9280\n"
"s_waitcnt vmcnt(8)\n"
"ds_write_b128 v13, v[184:187] offset:17408\n"
"ds_write_b128 v13, v[188:191] offset:18496\n"
"ds_write_b128 v13, v[248:251] offset:19584\n"
"ds_write_b128 v13, v[252:255] offset:20672\n"
"s_waitcnt lgkmcnt(0)\n"
"s_barrier\n"
"ds_read_b64_tr_b16 v[184:185], v14 offset:17408\n"
"ds_read_b64_tr_b16 v[186:187], v14 offset:17952\n"
"ds_read_b64_tr_b16 v[188:189], v14 offset:26112\n"
"ds_read_b64_tr_b16 v[190:191], v14 offset:26656\n"
"ds_read_b64_tr_b16 v[248:249], v14 offset:17440\n"
"ds_read_b64_tr_b16 v[250:251], v14 offset:17984\n"
"ds_read_b64_tr_b16 v[252:253], v14 offset:26144\n"
"ds_read_b64_tr_b16 v[254:255], v14 offset:26688\n"
"s_waitcnt vmcnt(4)\n"
"ds_write_b128 v13, v[160:163] offset:0\n"
"ds_write_b128 v13, v[164:167] offset:1088\n"
"ds_write_b128 v13, v[224:227] offset:2176\n"
"ds_write_b128 v13, v[228:231] offset:3264\n"
"s_waitcnt lgkmcnt(0)\n"
"s_barrier\n"
"s_mov_b32 s79, 0x1b0200\n"
"buffer_load_dwordx2 v[104:105], off, s[52:55], s79 sc1\n"
"s_sub_u32 s64, s34, 0x1a0000\n"
"s_lshl_b32 s64, s64, 5\n"
"s_add_u32 s64, s64, 0x1b2000\n"
"buffer_load_dwordx2 v[106:107], off, s[52:55], s64 sc1\n"
"ds_read_b64_tr_b16 v[160:161], v14 offset:0\n"
"ds_read_b64_tr_b16 v[162:163], v14 offset:544\n"
"ds_read_b64_tr_b16 v[164:165], v14 offset:8704\n"
"ds_read_b64_tr_b16 v[166:167], v14 offset:9248\n"
"ds_read_b64_tr_b16 v[224:225], v14 offset:32\n"
"ds_read_b64_tr_b16 v[226:227], v14 offset:576\n"
"ds_read_b64_tr_b16 v[228:229], v14 offset:8736\n"
"ds_read_b64_tr_b16 v[230:231], v14 offset:9280\n"
"s_waitcnt vmcnt(2)\n"
"ds_write_b128 v13, v[168:171] offset:17408\n"
"ds_write_b128 v13, v[172:175] offset:18496\n"
"ds_write_b128 v13, v[232:235] offset:19584\n"
"ds_write_b128 v13, v[236:239] offset:20672\n"
"s_waitcnt lgkmcnt(0)\n"
"s_barrier\n"
"ds_read_b64_tr_b16 v[168:169], v14 offset:17408\n"
"ds_read_b64_tr_b16 v[170:171], v14 offset:17952\n"
"ds_read_b64_tr_b16 v[172:173], v14 offset:26112\n"
"ds_read_b64_tr_b16 v[174:175], v14 offset:26656\n"
"ds_read_b64_tr_b16 v[232:233], v14 offset:17440\n"
"ds_read_b64_tr_b16 v[234:235], v14 offset:17984\n"
"ds_read_b64_tr_b16 v[236:237], v14 offset:26144\n"
"ds_read_b64_tr_b16 v[238:239], v14 offset:26688\n"
"s_waitcnt lgkmcnt(0)\n"
"ds_write_b128 v11, v[32:35] offset:8192\n"
"ds_write_b128 v11, v[36:39] offset:9216\n"
"ds_write_b128 v11, v[40:43] offset:10240\n"
"ds_write_b128 v11, v[44:47] offset:11264\n"
"ds_write_b128 v11, v[48:51] offset:12288\n"
"ds_write_b128 v11, v[52:55] offset:13312\n"
"ds_write_b128 v11, v[56:59] offset:14336\n"
"ds_write_b128 v11, v[60:63] offset:15360\n"
"s_branch L_sudone_%=\n"
"L_su3_%=:\n"
"s_xor_b32 s79, s84, 0x0\n"
"buffer_load_dwordx4 a[8:11], v12, s[56:59], s79 offen\n"
"s_xor_b32 s79, s84, 0x8000\n"
"buffer_load_dwordx4 a[12:15], v12, s[56:59], s79 offen\n"
"s_xor_b32 s79, s84, 0x10000\n"
"buffer_load_dwordx4 a[72:75], v12, s[56:59], s79 offen\n"
"s_xor_b32 s79, s84, 0x18000\n"
"buffer_load_dwordx4 a[76:79], v12, s[56:59], s79 offen\n"
"s_xor_b32 s79, s84, 0x80000\n"
"buffer_load_dwordx4 a[16:19], v12, s[56:59], s79 offen\n"
"s_xor_b32 s79, s84, 0x88000\n"
"buffer_load_dwordx4 a[20:23], v12, s[56:59], s79 offen\n"
"s_xor_b32 s79, s84, 0x90000\n"
"buffer_load_dwordx4 a[80:83], v12, s[56:59], s79 offen\n"
"s_xor_b32 s79, s84, 0x98000\n"
"buffer_load_dwordx4 a[84:87], v12, s[56:59], s79 offen\n"
"s_xor_b32 s79, s84, 0x100000\n"
"buffer_load_dwordx4 a[24:27], v12, s[56:59], s79 offen\n"
"s_xor_b32 s79, s84, 0x108000\n"
"buffer_load_dwordx4 a[28:31], v12, s[56:59], s79 offen\n"
"s_xor_b32 s79, s84, 0x110000\n"
"buffer_load_dwordx4 a[88:91], v12, s[56:59], s79 offen\n"
"s_xor_b32 s79, s84, 0x118000\n"
"buffer_load_dwordx4 a[92:95], v12, s[56:59], s79 offen\n"
"s_xor_b32 s79, s84, 0x180000\n"
"buffer_load_dwordx4 a[0:3], v12, s[56:59], s79 offen\n"
"s_xor_b32 s79, s84, 0x188000\n"
"buffer_load_dwordx4 a[4:7], v12, s[56:59], s79 offen\n"
"s_xor_b32 s79, s84, 0x190000\n"
"buffer_load_dwordx4 a[64:67], v12, s[56:59], s79 offen\n"
"s_xor_b32 s79, s84, 0x198000\n"
"buffer_load_dwordx4 a[68:71], v12, s[56:59], s79 offen\n"
"s_xor_b32 s79, s84, 0x200000\n"
"buffer_load_dwordx4 a[40:43], v12, s[56:59], s79 offen\n"
"s_xor_b32 s79, s84, 0x208000\n"
"buffer_load_dwordx4 a[44:47], v12, s[56:59], s79 offen\n"
"s_xor_b32 s79, s84, 0x210000\n"
"buffer_load_dwordx4 a[104:107], v12, s[56:59], s79 offen\n"
"s_xor_b32 s79, s84, 0x218000\n"
"buffer_load_dwordx4 a[108:111], v12, s[56:59], s79 offen\n"
"s_xor_b32 s79, s84, 0x280000\n"
"buffer_load_dwordx4 a[48:51], v12, s[56:59], s79 offen\n"
"s_xor_b32 s79, s84, 0x288000\n"
"buffer_load_dwordx4 a[52:55], v12, s[56:59], s79 offen\n"
"s_xor_b32 s79, s84, 0x290000\n"
"buffer_load_dwordx4 a[112:115], v12, s[56:59], s79 offen\n"
"s_xor_b32 s79, s84, 0x298000\n"
"buffer_load_dwordx4 a[116:119], v12, s[56:59], s79 offen\n"
"s_xor_b32 s79, s84, 0x300000\n"
"buffer_load_dwordx4 a[56:59], v12, s[56:59], s79 offen\n"
"s_xor_b32 s79, s84, 0x308000\n"
"buffer_load_dwordx4 a[60:63], v12, s[56:59], s79 offen\n"
"s_xor_b32 s79, s84, 0x310000\n"
"buffer_load_dwordx4 a[120:123], v12, s[56:59], s79 offen\n"
"s_xor_b32 s79, s84, 0x318000\n"
"buffer_load_dwordx4 a[124:127], v12, s[56:59], s79 offen\n"
"s_xor_b32 s79, s84, 0x380000\n"
"buffer_load_dwordx4 a[32:35], v12, s[56:59], s79 offen\n"
"s_xor_b32 s79, s84, 0x388000\n"
"buffer_load_dwordx4 a[36:39], v12, s[56:59], s79 offen\n"
"s_xor_b32 s79, s84, 0x390000\n"
"buffer_load_dwordx4 a[96:99], v12, s[56:59], s79 offen\n"
"s_xor_b32 s79, s84, 0x398000\n"
"buffer_load_dwordx4 a[100:103], v12, s[56:59], s79 offen\n"
"s_xor_b32 s79, s84, 0x0\n"
"buffer_load_dwordx4 a[136:139], v12, s[60:63], s79 offen\n"
"s_xor_b32 s79, s84, 0x8000\n"
"buffer_load_dwordx4 a[140:143], v12, s[60:63], s79 offen\n"
"s_xor_b32 s79, s84, 0x10000\n"
"buffer_load_dwordx4 a[200:203], v12, s[60:63], s79 offen\n"
"s_xor_b32 s79, s84, 0x18000\n"
"buffer_load_dwordx4 a[204:207], v12, s[60:63], s79 offen\n"
"s_xor_b32 s79, s84, 0x80000\n"
"buffer_load_dwordx4 a[144:147], v12, s[60:63], s79 offen\n"
"s_xor_b32 s79, s84, 0x88000\n"
"buffer_load_dwordx4 a[148:151], v12, s[60:63], s79 offen\n"
"s_xor_b32 s79, s84, 0x90000\n"
"buffer_load_dwordx4 a[208:211], v12, s[60:63], s79 offen\n"
"s_xor_b32 s79, s84, 0x98000\n"
"buffer_load_dwordx4 a[212:215], v12, s[60:63], s79 offen\n"
"s_waitcnt vmcnt(40)\n"
"ds_write_b128 v11, v[32:35] offset:0\n"
"ds_write_b128 v11, v[36:39] offset:1024\n"
"ds_write_b128 v11, v[40:43] offset:2048\n"
"ds_write_b128 v11, v[44:47] offset:3072\n"
"ds_write_b128 v11, v[48:51] offset:4096\n"
"ds_write_b128 v11, v[52:55] offset:5120\n"
"ds_write_b128 v11, v[56:59] offset:6144\n"
"ds_write_b128 v11, v[60:63] offset:7168\n"
"ds_write_b128 v11, v[64:67] offset:8192\n"
"ds_write_b128 v11, v[68:71] offset:9216\n"
"ds_write_b128 v11, v[72:75] offset:10240\n"
"ds_write_b128 v11, v[76:79] offset:11264\n"
"ds_write_b128 v11, v[80:83] offset:12288\n"
"ds_write_b128 v11, v[84:87] offset:13312\n"
"ds_write_b128 v11, v[88:91] offset:14336\n"
"ds_write_b128 v11, v[92:95] offset:15360\n"
"ds_read_b64_tr_b16 v[128:129], v16 offset:0\n"
"ds_read_b64_tr_b16 v[130:131], v16 offset:256\n"
"ds_read_b64_tr_b16 v[132:133], v16 offset:4096\n"
"ds_read_b64_tr_b16 v[134:135], v16 offset:4352\n"
"ds_read_b64_tr_b16 v[136:137], v16 offset:32\n"
"ds_read_b64_tr_b16 v[138:139], v16 offset:288\n"
"ds_read_b64_tr_b16 v[140:141], v16 offset:4128\n"
"ds_read_b64_tr_b16 v[142:143], v16 offset:4384\n"
"ds_read_b64_tr_b16 v[32:33], v16 offset:64\n"
"ds_read_b64_tr_b16 v[34:35], v16 offset:320\n"
"ds_read_b64_tr_b16 v[36:37], v16 offset:4160\n"
"ds_read_b64_tr_b16 v[38:39], v16 offset:4416\n"
"ds_read_b64_tr_b16 v[40:41], v16 offset:96\n"
"ds_read_b64_tr_b16 v[42:43], v16 offset:352\n"
"ds_read_b64_tr_b16 v[44:45], v16 offset:4192\n"
"ds_read_b64_tr_b16 v[46:47], v16 offset:4448\n"
"ds_read_b64_tr_b16 v[192:193], v16 offset:8192\n"
"ds_read_b64_tr_b16 v[194:195], v16 offset:8448\n"
"ds_read_b64_tr_b16 v[196:197], v16 offset:12288\n"
"ds_read_b64_tr_b16 v[198:199], v16 offset:12544\n"
"ds_read_b64_tr_b16 v[200:201], v16 offset:8224\n"
"ds_read_b64_tr_b16 v[202:203], v16 offset:8480\n"
"ds_read_b64_tr_b16 v[204:205], v16 offset:12320\n"
"ds_read_b64_tr_b16 v[206:207], v16 offset:12576\n"
"ds_read_b64_tr_b16 v[48:49], v16 offset:8256\n"
"ds_read_b64_tr_b16 v[50:51], v16 offset:8512\n"
"ds_read_b64_tr_b16 v[52:53], v16 offset:12352\n"
"ds_read_b64_tr_b16 v[54:55], v16 offset:12608\n"
"ds_read_b64_tr_b16 v[56:57], v16 offset:8288\n"
"ds_read_b64_tr_b16 v[58:59], v16 offset:8544\n"
"ds_read_b64_tr_b16 v[60:61], v16 offset:12384\n"
"ds_read_b64_tr_b16 v[62:63], v16 offset:12640\n"
"s_waitcnt lgkmcnt(0)\n"
"ds_write_b128 v11, v[32:35] offset:0\n"
"ds_write_b128 v11, v[36:39] offset:1024\n"
"ds_write_b128 v11, v[40:43] offset:2048\n"
"ds_write_b128 v11, v[44:47] offset:3072\n"
"ds_write_b128 v11, v[48:51] offset:4096\n"
"ds_write_b128 v11, v[52:55] offset:5120\n"
"ds_write_b128 v11, v[56:59] offset:6144\n"
"ds_write_b128 v11, v[60:63] offset:7168\n"
"s_xor_b32 s79, s84, 0x100000\n"
"buffer_load_dwordx4 a[152:155], v12, s[60:63], s79 offen\n"
"s_xor_b32 s79, s84, 0x108000\n"
"buffer_load_dwordx4 a[156:159], v12, s[60:63], s79 offen\n"
"s_xor_b32 s79, s84, 0x110000\n"
"buffer_load_dwordx4 a[216:219], v12, s[60:63], s79 offen\n"
"s_xor_b32 s79, s84, 0x118000\n"
"buffer_load_dwordx4 a[220:223], v12, s[60:63], s79 offen\n"
"s_waitcnt vmcnt(40)\n"
"ds_write_b128 v13, a[8:11] offset:0\n"
"ds_write_b128 v13, a[12:15] offset:1088\n"
"ds_write_b128 v13, a[72:75] offset:2176\n"
"ds_write_b128 v13, a[76:79] offset:3264\n"
"s_waitcnt lgkmcnt(0)\n"
"s_barrier\n"
"ds_read_b64_tr_b16 a[8:9], v14 offset:0\n"
"ds_read_b64_tr_b16 a[10:11], v14 offset:544\n"
"ds_read_b64_tr_b16 a[12:13], v14 offset:8704\n"
"ds_read_b64_tr_b16 a[14:15], v14 offset:9248\n"
"ds_read_b64_tr_b16 a[72:73], v14 offset:32\n"
"ds_read_b64_tr_b16 a[74:75], v14 offset:576\n"
"ds_read_b64_tr_b16 a[76:77], v14 offset:8736\n"
"ds_read_b64_tr_b16 a[78:79], v14 offset:9280\n"
"s_xor_b32 s79, s84, 0x180000\n"
"buffer_load_dwordx4 a[128:131], v12, s[60:63], s79 offen\n"
"s_xor_b32 s79, s84, 0x188000\n"
"buffer_load_dwordx4 a[132:135], v12, s[60:63], s79 offen\n"
"s_xor_b32 s79, s84, 0x190000\n"
"buffer_load_dwordx4 a[192:195], v12, s[60:63], s79 offen\n"
"s_xor_b32 s79, s84, 0x198000\n"
"buffer_load_dwordx4 a[196:199], v12, s[60:63], s79 offen\n"
"s_waitcnt vmcnt(40)\n"
"ds_write_b128 v13, a[16:19] offset:17408\n"
"ds_write_b128 v13, a[20:23] offset:18496\n"
"ds_write_b128 v13, a[80:83] offset:19584\n"
"ds_write_b128 v13, a[84:87] offset:20672\n"
"s_waitcnt lgkmcnt(0)\n"
"s_barrier\n"
"ds_read_b64_tr_b16 a[16:17], v14 offset:17408\n"
"ds_read_b64_tr_b16 a[18:19], v14 offset:17952\n"
"ds_read_b64_tr_b16 a[20:21], v14 offset:26112\n"
"ds_read_b64_tr_b16 a[22:23], v14 offset:26656\n"
"ds_read_b64_tr_b16 a[80:81], v14 offset:17440\n"
"ds_read_b64_tr_b16 a[82:83], v14 offset:17984\n"
"ds_read_b64_tr_b16 a[84:85], v14 offset:26144\n"
"ds_read_b64_tr_b16 a[86:87], v14 offset:26688\n"
"s_xor_b32 s79, s84, 0x200000\n"
"buffer_load_dwordx4 a[168:171], v12, s[60:63], s79 offen\n"
"s_xor_b32 s79, s84, 0x208000\n"
"buffer_load_dwordx4 a[172:175], v12, s[60:63], s79 offen\n"
"s_xor_b32 s79, s84, 0x210000\n"
"buffer_load_dwordx4 a[232:235], v12, s[60:63], s79 offen\n"
"s_xor_b32 s79, s84, 0x218000\n"
"buffer_load_dwordx4 a[236:239], v12, s[60:63], s79 offen\n"
"s_waitcnt vmcnt(40)\n"
"ds_write_b128 v13, a[24:27] offset:0\n"
"ds_write_b128 v13, a[28:31] offset:1088\n"
"ds_write_b128 v13, a[88:91] offset:2176\n"
"ds_write_b128 v13, a[92:95] offset:3264\n"
"s_waitcnt lgkmcnt(0)\n"
"s_barrier\n"
"ds_read_b64_tr_b16 a[24:25], v14 offset:0\n"
"ds_read_b64_tr_b16 a[26:27], v14 offset:544\n"
"ds_read_b64_tr_b16 a[28:29], v14 offset:8704\n"
"ds_read_b64_tr_b16 a[30:31], v14 offset:9248\n"
"ds_read_b64_tr_b16 a[88:89], v14 offset:32\n"
"ds_read_b64_tr_b16 a[90:91], v14 offset:576\n"
"ds_read_b64_tr_b16 a[92:93], v14 offset:8736\n"
"ds_read_b64_tr_b16 a[94:95], v14 offset:9280\n"
"s_xor_b32 s79, s84, 0x280000\n"
"buffer_load_dwordx4 a[176:179], v12, s[60:63], s79 offen\n"
"s_xor_b32 s79, s84, 0x288000\n"
"buffer_load_dwordx4 a[180:183], v12, s[60:63], s79 offen\n"
"s_xor_b32 s79, s84, 0x290000\n"
"buffer_load_dwordx4 a[240:243], v12, s[60:63], s79 offen\n"
"s_xor_b32 s79, s84, 0x298000\n"
"buffer_load_dwordx4 a[244:247], v12, s[60:63], s79 offen\n"
"s_waitcnt vmcnt(40)\n"
"ds_write_b128 v13, a[0:3] offset:17408\n"
"ds_write_b128 v13, a[4:7] offset:18496\n"
"ds_write_b128 v13, a[64:67] offset:19584\n"
"ds_write_b128 v13, a[68:71] offset:20672\n"
"s_waitcnt lgkmcnt(0)\n"
"s_barrier\n"
"ds_read_b64_tr_b16 a[0:1], v14 offset:17408\n"
"ds_read_b64_tr_b16 a[2:3], v14 offset:17952\n"
"ds_read_b64_tr_b16 a[4:5], v14 offset:26112\n"
"ds_read_b64_tr_b16 a[6:7], v14 offset:26656\n"
"ds_read_b64_tr_b16 a[64:65], v14 offset:17440\n"
"ds_read_b64_tr_b16 a[66:67], v14 offset:17984\n"
"ds_read_b64_tr_b16 a[68:69], v14 offset:26144\n"
"ds_read_b64_tr_b16 a[70:71], v14 offset:26688\n"
"s_xor_b32 s79, s84, 0x300000\n"
"buffer_load_dwordx4 a[184:187], v12, s[60:63], s79 offen\n"
"s_xor_b32 s79, s84, 0x308000\n"
"buffer_load_dwordx4 a[188:191], v12, s[60:63], s79 offen\n"
"s_xor_b32 s79, s84, 0x310000\n"
"buffer_load_dwordx4 a[248:251], v12, s[60:63], s79 offen\n"
"s_xor_b32 s79, s84, 0x318000\n"
"buffer_load_dwordx4 a[252:255], v12, s[60:63], s79 offen\n"
"s_waitcnt vmcnt(40)\n"
"ds_write_b128 v13, a[40:43] offset:0\n"
"ds_write_b128 v13, a[44:47] offset:1088\n"
"ds_write_b128 v13, a[104:107] offset:2176\n"
"ds_write_b128 v13, a[108:111] offset:3264\n"
"s_waitcnt lgkmcnt(0)\n"
"s_barrier\n"
"ds_read_b64_tr_b16 a[40:41], v14 offset:0\n"
"ds_read_b64_tr_b16 a[42:43], v14 offset:544\n"
"ds_read_b64_tr_b16 a[44:45], v14 offset:8704\n"
"ds_read_b64_tr_b16 a[46:47], v14 offset:9248\n"
"ds_read_b64_tr_b16 a[104:105], v14 offset:32\n"
"ds_read_b64_tr_b16 a[106:107], v14 offset:576\n"
"ds_read_b64_tr_b16 a[108:109], v14 offset:8736\n"
"ds_read_b64_tr_b16 a[110:111], v14 offset:9280\n"
"s_xor_b32 s79, s84, 0x380000\n"
"buffer_load_dwordx4 a[160:163], v12, s[60:63], s79 offen\n"
"s_xor_b32 s79, s84, 0x388000\n"
"buffer_load_dwordx4 a[164:167], v12, s[60:63], s79 offen\n"
"s_xor_b32 s79, s84, 0x390000\n"
"buffer_load_dwordx4 a[224:227], v12, s[60:63], s79 offen\n"
"s_xor_b32 s79, s84, 0x398000\n"
"buffer_load_dwordx4 a[228:231], v12, s[60:63], s79 offen\n"
"s_waitcnt vmcnt(40)\n"
"ds_write_b128 v13, a[48:51] offset:17408\n"
"ds_write_b128 v13, a[52:55] offset:18496\n"
"ds_write_b128 v13, a[112:115] offset:19584\n"
"ds_write_b128 v13, a[116:119] offset:20672\n"
"s_waitcnt lgkmcnt(0)\n"
"s_barrier\n"
"ds_read_b64_tr_b16 a[48:49], v14 offset:17408\n"
"ds_read_b64_tr_b16 a[50:51], v14 offset:17952\n"
"ds_read_b64_tr_b16 a[52:53], v14 offset:26112\n"
"ds_read_b64_tr_b16 a[54:55], v14 offset:26656\n"
"ds_read_b64_tr_b16 a[112:113], v14 offset:17440\n"
"ds_read_b64_tr_b16 a[114:115], v14 offset:17984\n"
"ds_read_b64_tr_b16 a[116:117], v14 offset:26144\n"
"ds_read_b64_tr_b16 a[118:119], v14 offset:26688\n"
"s_xor_b32 s79, s84, 0x0\n"
"buffer_load_dwordx4 v[48:51], v12, s[64:67], s79 offen\n"
"s_xor_b32 s79, s84, 0x8000\n"
"buffer_load_dwordx4 v[52:55], v12, s[64:67], s79 offen\n"
"s_xor_b32 s79, s84, 0x10000\n"
"buffer_load_dwordx4 v[56:59], v12, s[64:67], s79 offen\n"
"s_xor_b32 s79, s84, 0x18000\n"
"buffer_load_dwordx4 v[60:63], v12, s[64:67], s79 offen\n"
"s_waitcnt vmcnt(40)\n"
"ds_write_b128 v13, a[56:59] offset:0\n"
"ds_write_b128 v13, a[60:63] offset:1088\n"
"ds_write_b128 v13, a[120:123] offset:2176\n"
"ds_write_b128 v13, a[124:127] offset:3264\n"
"s_waitcnt lgkmcnt(0)\n"
"s_barrier\n"
"ds_read_b64_tr_b16 a[56:57], v14 offset:0\n"
"ds_read_b64_tr_b16 a[58:59], v14 offset:544\n"
"ds_read_b64_tr_b16 a[60:61], v14 offset:8704\n"
"ds_read_b64_tr_b16 a[62:63], v14 offset:9248\n"
"ds_read_b64_tr_b16 a[120:121], v14 offset:32\n"
"ds_read_b64_tr_b16 a[122:123], v14 offset:576\n"
"ds_read_b64_tr_b16 a[124:125], v14 offset:8736\n"
"ds_read_b64_tr_b16 a[126:127], v14 offset:9280\n"
"s_xor_b32 s79, s84, 0x80000\n"
"buffer_load_dwordx4 v[144:147], v12, s[64:67], s79 offen\n"
"s_xor_b32 s79, s84, 0x88000\n"
"buffer_load_dwordx4 v[148:151], v12, s[64:67], s79 offen\n"
"s_xor_b32 s79, s84, 0x90000\n"
"buffer_load_dwordx4 v[208:211], v12, s[64:67], s79 offen\n"
"s_xor_b32 s79, s84, 0x98000\n"
"buffer_load_dwordx4 v[212:215], v12, s[64:67], s79 offen\n"
"s_waitcnt vmcnt(40)\n"
"ds_write_b128 v13, a[32:35] offset:17408\n"
"ds_write_b128 v13, a[36:39] offset:18496\n"
"ds_write_b128 v13, a[96:99] offset:19584\n"
"ds_write_b128 v13, a[100:103] offset:20672\n"
"s_waitcnt lgkmcnt(0)\n"
"s_barrier\n"
"ds_read_b64_tr_b16 a[32:33], v14 offset:17408\n"
"ds_read_b64_tr_b16 a[34:35], v14 offset:17952\n"
"ds_read_b64_tr_b16 a[36:37], v14 offset:26112\n"
"ds_read_b64_tr_b16 a[38:39], v14 offset:26656\n"
"ds_read_b64_tr_b16 a[96:97], v14 offset:17440\n"
"ds_read_b64_tr_b16 a[98:99], v14 offset:17984\n"
"ds_read_b64_tr_b16 a[100:101], v14 offset:26144\n"
"ds_read_b64_tr_b16 a[102:103], v14 offset:26688\n"
"s_xor_b32 s79, s84, 0x100000\n"
"buffer_load_dwordx4 v[152:155], v12, s[64:67], s79 offen\n"
"s_xor_b32 s79, s84, 0x108000\n"
"buffer_load_dwordx4 v[156:159], v12, s[64:67], s79 offen\n"
"s_xor_b32 s79, s84, 0x110000\n"
"buffer_load_dwordx4 v[216:219], v12, s[64:67], s79 offen\n"
"s_xor_b32 s79, s84, 0x118000\n"
"buffer_load_dwordx4 v[220:223], v12, s[64:67], s79 offen\n"
"s_waitcnt vmcnt(40)\n"
"ds_write_b128 v13, a[136:139] offset:0\n"
"ds_write_b128 v13, a[140:143] offset:1088\n"
"ds_write_b128 v13, a[200:203] offset:2176\n"
"ds_write_b128 v13, a[204:207] offset:3264\n"
"s_waitcnt lgkmcnt(0)\n"
"s_barrier\n"
"ds_read_b64_tr_b16 a[136:137], v14 offset:0\n"
"ds_read_b64_tr_b16 a[138:139], v14 offset:544\n"
"ds_read_b64_tr_b16 a[140:141], v14 offset:8704\n"
"ds_read_b64_tr_b16 a[142:143], v14 offset:9248\n"
"ds_read_b64_tr_b16 a[200:201], v14 offset:32\n"
"ds_read_b64_tr_b16 a[202:203], v14 offset:576\n"
"ds_read_b64_tr_b16 a[204:205], v14 offset:8736\n"
"ds_read_b64_tr_b16 a[206:207], v14 offset:9280\n"
"s_xor_b32 s79, s84, 0x180000\n"
"buffer_load_dwordx4 v[32:35], v12, s[64:67], s79 offen\n"
"s_xor_b32 s79, s84, 0x188000\n"
"buffer_load_dwordx4 v[36:39], v12, s[64:67], s79 offen\n"
"s_xor_b32 s79, s84, 0x190000\n"
"buffer_load_dwordx4 v[40:43], v12, s[64:67], s79 offen\n"
"s_xor_b32 s79, s84, 0x198000\n"
"buffer_load_dwordx4 v[44:47], v12, s[64:67], s79 offen\n"
"s_waitcnt vmcnt(40)\n"
"ds_write_b128 v13, a[144:147] offset:17408\n"
"ds_write_b128 v13, a[148:151] offset:18496\n"
"ds_write_b128 v13, a[208:211] offset:19584\n"
"ds_write_b128 v13, a[212:215] offset:20672\n"
"s_waitcnt lgkmcnt(0)\n"
"s_barrier\n"
"ds_read_b64_tr_b16 a[144:145], v14 offset:17408\n"
"ds_read_b64_tr_b16 a[146:147], v14 offset:17952\n"
"ds_read_b64_tr_b16 a[148:149], v14 offset:26112\n"
"ds_read_b64_tr_b16 a[150:151], v14 offset:26656\n"
"ds_read_b64_tr_b16 a[208:209], v14 offset:17440\n"
"ds_read_b64_tr_b16 a[210:211], v14 offset:17984\n"
"ds_read_b64_tr_b16 a[212:213], v14 offset:26144\n"
"ds_read_b64_tr_b16 a[214:215], v14 offset:26688\n"
"s_xor_b32 s79, s84, 0x200000\n"
"buffer_load_dwordx4 v[168:171], v12, s[64:67], s79 offen\n"
"s_xor_b32 s79, s84, 0x208000\n"
"buffer_load_dwordx4 v[172:175], v12, s[64:67], s79 offen\n"
"s_xor_b32 s79, s84, 0x210000\n"
"buffer_load_dwordx4 v[232:235], v12, s[64:67], s79 offen\n"
"s_xor_b32 s79, s84, 0x218000\n"
"buffer_load_dwordx4 v[236:239], v12, s[64:67], s79 offen\n"
"s_waitcnt vmcnt(40)\n"
"ds_write_b128 v13, a[152:155] offset:0\n"
"ds_write_b128 v13, a[156:159] offset:1088\n"
"ds_write_b128 v13, a[216:219] offset:2176\n"
"ds_write_b128 v13, a[220:223] offset:3264\n"
"s_waitcnt lgkmcnt(0)\n"
"s_barrier\n"
"ds_read_b64_tr_b16 a[152:153], v14 offset:0\n"
"ds_read_b64_tr_b16 a[154:155], v14 offset:544\n"
"ds_read_b64_tr_b16 a[156:157], v14 offset:8704\n"
"ds_read_b64_tr_b16 a[158:159], v14 offset:9248\n"
"ds_read_b64_tr_b16 a[216:217], v14 offset:32\n"
"ds_read_b64_tr_b16 a[218:219], v14 offset:576\n"
"ds_read_b64_tr_b16 a[220:221], v14 offset:8736\n"
"ds_read_b64_tr_b16 a[222:223], v14 offset:9280\n"
"s_xor_b32 s79, s84, 0x280000\n"
"buffer_load_dwordx4 v[176:179], v12, s[64:67], s79 offen\n"
"s_xor_b32 s79, s84, 0x288000\n"
"buffer_load_dwordx4 v[180:183], v12, s[64:67], s79 offen\n"
"s_xor_b32 s79, s84, 0x290000\n"
"buffer_load_dwordx4 v[240:243], v12, s[64:67], s79 offen\n"
"s_xor_b32 s79, s84, 0x298000\n"
"buffer_load_dwordx4 v[244:247], v12, s[64:67], s79 offen\n"
"s_waitcnt vmcnt(40)\n"
"ds_write_b128 v13, a[128:131] offset:17408\n"
"ds_write_b128 v13, a[132:135] offset:18496\n"
"ds_write_b128 v13, a[192:195] offset:19584\n"
"ds_write_b128 v13, a[196:199] offset:20672\n"
"s_waitcnt lgkmcnt(0)\n"
"s_barrier\n"
"ds_read_b64_tr_b16 a[128:129], v14 offset:17408\n"
"ds_read_b64_tr_b16 a[130:131], v14 offset:17952\n"
"ds_read_b64_tr_b16 a[132:133], v14 offset:26112\n"
"ds_read_b64_tr_b16 a[134:135], v14 offset:26656\n"
"ds_read_b64_tr_b16 a[192:193], v14 offset:17440\n"
"ds_read_b64_tr_b16 a[194:195], v14 offset:17984\n"
"ds_read_b64_tr_b16 a[196:197], v14 offset:26144\n"
"ds_read_b64_tr_b16 a[198:199], v14 offset:26688\n"
"s_xor_b32 s79, s84, 0x300000\n"
"buffer_load_dwordx4 v[184:187], v12, s[64:67], s79 offen\n"
"s_xor_b32 s79, s84, 0x308000\n"
"buffer_load_dwordx4 v[188:191], v12, s[64:67], s79 offen\n"
"s_xor_b32 s79, s84, 0x310000\n"
"buffer_load_dwordx4 v[248:251], v12, s[64:67], s79 offen\n"
"s_xor_b32 s79, s84, 0x318000\n"
"buffer_load_dwordx4 v[252:255], v12, s[64:67], s79 offen\n"
"s_waitcnt vmcnt(40)\n"
"ds_write_b128 v13, a[168:171] offset:0\n"
"ds_write_b128 v13, a[172:175] offset:1088\n"
"ds_write_b128 v13, a[232:235] offset:2176\n"
"ds_write_b128 v13, a[236:239] offset:3264\n"
"s_waitcnt lgkmcnt(0)\n"
"s_barrier\n"
"ds_read_b64_tr_b16 a[168:169], v14 offset:0\n"
"ds_read_b64_tr_b16 a[170:171], v14 offset:544\n"
"ds_read_b64_tr_b16 a[172:173], v14 offset:8704\n"
"ds_read_b64_tr_b16 a[174:175], v14 offset:9248\n"
"ds_read_b64_tr_b16 a[232:233], v14 offset:32\n"
"ds_read_b64_tr_b16 a[234:235], v14 offset:576\n"
"ds_read_b64_tr_b16 a[236:237], v14 offset:8736\n"
"ds_read_b64_tr_b16 a[238:239], v14 offset:9280\n"
"s_xor_b32 s79, s84, 0x380000\n"
"buffer_load_dwordx4 v[160:163], v12, s[64:67], s79 offen\n"
"s_xor_b32 s79, s84, 0x388000\n"
"buffer_load_dwordx4 v[164:167], v12, s[64:67], s79 offen\n"
"s_xor_b32 s79, s84, 0x390000\n"
"buffer_load_dwordx4 v[224:227], v12, s[64:67], s79 offen\n"
"s_xor_b32 s79, s84, 0x398000\n"
"buffer_load_dwordx4 v[228:231], v12, s[64:67], s79 offen\n"
"s_waitcnt vmcnt(40)\n"
"ds_write_b128 v13, a[176:179] offset:17408\n"
"ds_write_b128 v13, a[180:183] offset:18496\n"
"ds_write_b128 v13, a[240:243] offset:19584\n"
"ds_write_b128 v13, a[244:247] offset:20672\n"
"s_waitcnt lgkmcnt(0)\n"
"s_barrier\n"
"ds_read_b64_tr_b16 a[176:177], v14 offset:17408\n"
"ds_read_b64_tr_b16 a[178:179], v14 offset:17952\n"
"ds_read_b64_tr_b16 a[180:181], v14 offset:26112\n"
"ds_read_b64_tr_b16 a[182:183], v14 offset:26656\n"
"ds_read_b64_tr_b16 a[240:241], v14 offset:17440\n"
"ds_read_b64_tr_b16 a[242:243], v14 offset:17984\n"
"ds_read_b64_tr_b16 a[244:245], v14 offset:26144\n"
"ds_read_b64_tr_b16 a[246:247], v14 offset:26688\n"
"s_waitcnt vmcnt(36)\n"
"ds_write_b128 v13, a[184:187] offset:0\n"
"ds_write_b128 v13, a[188:191] offset:1088\n"
"ds_write_b128 v13, a[248:251] offset:2176\n"
"ds_write_b128 v13, a[252:255] offset:3264\n"
"s_waitcnt lgkmcnt(0)\n"
"s_barrier\n"
"ds_read_b64_tr_b16 a[184:185], v14 offset:0\n"
"ds_read_b64_tr_b16 a[186:187], v14 offset:544\n"
"ds_read_b64_tr_b16 a[188:189], v14 offset:8704\n"
"ds_read_b64_tr_b16 a[190:191], v14 offset:9248\n"
"ds_read_b64_tr_b16 a[248:249], v14 offset:32\n"
"ds_read_b64_tr_b16 a[250:251], v14 offset:576\n"
"ds_read_b64_tr_b16 a[252:253], v14 offset:8736\n"
"ds_read_b64_tr_b16 a[254:255], v14 offset:9280\n"
"s_waitcnt vmcnt(32)\n"
"ds_write_b128 v13, a[160:163] offset:17408\n"
"ds_write_b128 v13, a[164:167] offset:18496\n"
"ds_write_b128 v13, a[224:227] offset:19584\n"
"ds_write_b128 v13, a[228:231] offset:20672\n"
"s_waitcnt lgkmcnt(0)\n"
"s_barrier\n"
"ds_read_b64_tr_b16 a[160:161], v14 offset:17408\n"
"ds_read_b64_tr_b16 a[162:163], v14 offset:17952\n"
"ds_read_b64_tr_b16 a[164:165], v14 offset:26112\n"
"ds_read_b64_tr_b16 a[166:167], v14 offset:26656\n"
"ds_read_b64_tr_b16 a[224:225], v14 offset:17440\n"
"ds_read_b64_tr_b16 a[226:227], v14 offset:17984\n"
"ds_read_b64_tr_b16 a[228:229], v14 offset:26144\n"
"ds_read_b64_tr_b16 a[230:231], v14 offset:26688\n"
"s_waitcnt vmcnt(28)\n"
"ds_write_b128 v13, v[48:51] offset:0\n"
"ds_write_b128 v13, v[52:55] offset:1088\n"
"ds_write_b128 v13, v[56:59] offset:2176\n"
"ds_write_b128 v13, v[60:63] offset:3264\n"
"s_waitcnt lgkmcnt(0)\n"
"s_barrier\n"
"ds_read_b64_tr_b16 v[48:49], v14 offset:0\n"
"ds_read_b64_tr_b16 v[50:51], v14 offset:544\n"
"ds_read_b64_tr_b16 v[52:53], v14 offset:8704\n"
"ds_read_b64_tr_b16 v[54:55], v14 offset:9248\n"
"ds_read_b64_tr_b16 v[56:57], v14 offset:32\n"
"ds_read_b64_tr_b16 v[58:59], v14 offset:576\n"
"ds_read_b64_tr_b16 v[60:61], v14 offset:8736\n"
"ds_read_b64_tr_b16 v[62:63], v14 offset:9280\n"
"s_waitcnt vmcnt(24)\n"
"ds_write_b128 v13, v[144:147] offset:17408\n"
"ds_write_b128 v13, v[148:151] offset:18496\n"
"ds_write_b128 v13, v[208:211] offset:19584\n"
"ds_write_b128 v13, v[212:215] offset:20672\n"
"s_waitcnt lgkmcnt(0)\n"
"s_barrier\n"
"ds_read_b64_tr_b16 v[144:145], v14 offset:17408\n"
"ds_read_b64_tr_b16 v[146:147], v14 offset:17952\n"
"ds_read_b64_tr_b16 v[148:149], v14 offset:26112\n"
"ds_read_b64_tr_b16 v[150:151], v14 offset:26656\n"
"ds_read_b64_tr_b16 v[208:209], v14 offset:17440\n"
"ds_read_b64_tr_b16 v[210:211], v14 offset:17984\n"
"ds_read_b64_tr_b16 v[212:213], v14 offset:26144\n"
"ds_read_b64_tr_b16 v[214:215], v14 offset:26688\n"
"s_waitcnt vmcnt(20)\n"
"ds_write_b128 v13, v[152:155] offset:0\n"
"ds_write_b128 v13, v[156:159] offset:1088\n"
"ds_write_b128 v13, v[216:219] offset:2176\n"
"ds_write_b128 v13, v[220:223] offset:3264\n"
"s_waitcnt lgkmcnt(0)\n"
"s_barrier\n"
"ds_read_b64_tr_b16 v[152:153], v14 offset:0\n"
"ds_read_b64_tr_b16 v[154:155], v14 offset:544\n"
"ds_read_b64_tr_b16 v[156:157], v14 offset:8704\n"
"ds_read_b64_tr_b16 v[158:159], v14 offset:9248\n"
"ds_read_b64_tr_b16 v[216:217], v14 offset:32\n"
"ds_read_b64_tr_b16 v[218:219], v14 offset:576\n"
"ds_read_b64_tr_b16 v[220:221], v14 offset:8736\n"
"ds_read_b64_tr_b16 v[222:223], v14 offset:9280\n"
"s_waitcnt vmcnt(16)\n"
"ds_write_b128 v13, v[32:35] offset:17408\n"
"ds_write_b128 v13, v[36:39] offset:18496\n"
"ds_write_b128 v13, v[40:43] offset:19584\n"
"ds_write_b128 v13, v[44:47] offset:20672\n"
"s_waitcnt lgkmcnt(0)\n"
"s_barrier\n"
"ds_read_b64_tr_b16 v[32:33], v14 offset:17408\n"
"ds_read_b64_tr_b16 v[34:35], v14 offset:17952\n"
"ds_read_b64_tr_b16 v[36:37], v14 offset:26112\n"
"ds_read_b64_tr_b16 v[38:39], v14 offset:26656\n"
"ds_read_b64_tr_b16 v[40:41], v14 offset:17440\n"
"ds_read_b64_tr_b16 v[42:43], v14 offset:17984\n"
"ds_read_b64_tr_b16 v[44:45], v14 offset:26144\n"
"ds_read_b64_tr_b16 v[46:47], v14 offset:26688\n"
"s_waitcnt vmcnt(12)\n"
"ds_write_b128 v13, v[168:171] offset:0\n"
"ds_write_b128 v13, v[172:175] offset:1088\n"
"ds_write_b128 v13, v[232:235] offset:2176\n"
"ds_write_b128 v13, v[236:239] offset:3264\n"
"s_waitcnt lgkmcnt(0)\n"
"s_barrier\n"
"ds_read_b64_tr_b16 v[168:169], v14 offset:0\n"
"ds_read_b64_tr_b16 v[170:171], v14 offset:544\n"
"ds_read_b64_tr_b16 v[172:173], v14 offset:8704\n"
"ds_read_b64_tr_b16 v[174:175], v14 offset:9248\n"
"ds_read_b64_tr_b16 v[232:233], v14 offset:32\n"
"ds_read_b64_tr_b16 v[234:235], v14 offset:576\n"
"ds_read_b64_tr_b16 v[236:237], v14 offset:8736\n"
"ds_read_b64_tr_b16 v[238:239], v14 offset:9280\n"
"s_waitcnt vmcnt(8)\n"
"ds_write_b128 v13, v[176:179] offset:17408\n"
"ds_write_b128 v13, v[180:183] offset:18496\n"
"ds_write_b128 v13, v[240:243] offset:19584\n"
"ds_write_b128 v13, v[244:247] offset:20672\n"
"s_waitcnt lgkmcnt(0)\n"
"s_barrier\n"
"ds_read_b64_tr_b16 v[176:177], v14 offset:17408\n"
"ds_read_b64_tr_b16 v[178:179], v14 offset:17952\n"
"ds_read_b64_tr_b16 v[180:181], v14 offset:26112\n"
"ds_read_b64_tr_b16 v[182:183], v14 offset:26656\n"
"ds_read_b64_tr_b16 v[240:241], v14 offset:17440\n"
"ds_read_b64_tr_b16 v[242:243], v14 offset:17984\n"
"ds_read_b64_tr_b16 v[244:245], v14 offset:26144\n"
"ds_read_b64_tr_b16 v[246:247], v14 offset:26688\n"
"s_waitcnt vmcnt(4)\n"
"ds_write_b128 v13, v[184:187] offset:0\n"
"ds_write_b128 v13, v[188:191] offset:1088\n"
"ds_write_b128 v13, v[248:251] offset:2176\n"
"ds_write_b128 v13, v[252:255] offset:3264\n"
"s_waitcnt lgkmcnt(0)\n"
"s_barrier\n"
"s_mov_b32 s79, 0x1b0200\n"
"buffer_load_dwordx2 v[104:105], off, s[52:55], s79 sc1\n"
"s_sub_u32 s64, s34, 0x1a0000\n"
"s_lshl_b32 s64, s64, 5\n"
"s_add_u32 s64, s64, 0x1b2000\n"
"buffer_load_dwordx2 v[106:107], off, s[52:55], s64 sc1\n"
"ds_read_b64_tr_b16 v[184:185], v14 offset:0\n"
"ds_read_b64_tr_b16 v[186:187], v14 offset:544\n"
"ds_read_b64_tr_b16 v[188:189], v14 offset:8704\n"
"ds_read_b64_tr_b16 v[190:191], v14 offset:9248\n"
"ds_read_b64_tr_b16 v[248:249], v14 offset:32\n"
"ds_read_b64_tr_b16 v[250:251], v14 offset:576\n"
"ds_read_b64_tr_b16 v[252:253], v14 offset:8736\n"
"ds_read_b64_tr_b16 v[254:255], v14 offset:9280\n"
"s_waitcnt vmcnt(2)\n"
"ds_write_b128 v13, v[160:163] offset:17408\n"
"ds_write_b128 v13, v[164:167] offset:18496\n"
"ds_write_b128 v13, v[224:227] offset:19584\n"
"ds_write_b128 v13, v[228:231] offset:20672\n"
"s_waitcnt lgkmcnt(0)\n"
"s_barrier\n"
"ds_read_b64_tr_b16 v[160:161], v14 offset:17408\n"
"ds_read_b64_tr_b16 v[162:163], v14 offset:17952\n"
"ds_read_b64_tr_b16 v[164:165], v14 offset:26112\n"
"ds_read_b64_tr_b16 v[166:167], v14 offset:26656\n"
"ds_read_b64_tr_b16 v[224:225], v14 offset:17440\n"
"ds_read_b64_tr_b16 v[226:227], v14 offset:17984\n"
"ds_read_b64_tr_b16 v[228:229], v14 offset:26144\n"
"ds_read_b64_tr_b16 v[230:231], v14 offset:26688\n"
"s_waitcnt lgkmcnt(0)\n"
"ds_write_b128 v11, v[32:35] offset:8192\n"
"ds_write_b128 v11, v[36:39] offset:9216\n"
"ds_write_b128 v11, v[40:43] offset:10240\n"
"ds_write_b128 v11, v[44:47] offset:11264\n"
"ds_write_b128 v11, v[48:51] offset:12288\n"
"ds_write_b128 v11, v[52:55] offset:13312\n"
"ds_write_b128 v11, v[56:59] offset:14336\n"
"ds_write_b128 v11, v[60:63] offset:15360\n"
"L_sudone_%=:\n"
"s_mov_b32 s72, 0\n"
"s_mov_b32 s73, 0\n"
"s_mov_b32 s74, 0x3f805a82\n"
"s_mov_b32 s75, 0\n"
"s_mov_b32 s80, s34\n"
"v_lshl_add_u32 v123, s48, 10, v0\n"
"s_xor_b32 s84, s28, 0x1000\n"
"s_lshr_b32 s85, s50, 8\n"
"s_and_b32 s50, s50, 0xff\n"
"s_lshr_b32 s70, s51, 8\n"
"s_and_b32 s51, s51, 0xff\n"
"s_mov_b32 s79, 0x1b0200\n"
"s_add_u32 s26, s26, 1\n"
"s_addc_u32 s27, s27, 0\n"
"s_memtime s[98:99]\n"
"s_memrealtime s[66:67]\n"
"s_waitcnt lgkmcnt(0)\n"
"s_sub_u32 s22, s98, s22\n"
"s_sub_u32 s24, s66, s24\n"
"v_cvt_f32_u32 v18, s22\n"
"v_cvt_f32_u32 v19, s24\n"
"v_cvt_f32_u32 v20, s70\n"
"v_rcp_f32 v19, v19\n"
"s_nop 0\n"
"v_mul_f32 v18, v18, v19\n"
"v_add_f32 v18, 0xc1bc0000, v18\n"
"v_mov_b32 v19, 0x3fb33333\n"
"v_fma_f32 v18, v18, v19, v20\n"
"v_add_f32 v18, 0.5, v18\n"
"v_max_f32 v18, 0, v18\n"
"v_min_f32 v18, 0x42800000, v18\n"
"v_cvt_u32_f32 v18, v18\n"
"s_nop 0\n"
"v_readfirstlane_b32 s70, v18\n"
"s_branch L_selchk_%=\n"
"L_sel_%=:\n"
"s_sleep 1\n"
"s_add_u32 s75, s75, 1\n"
"s_cmp_ge_u32 s75, 400000\n"
"s_cbranch_scc1 L_exit_%=\n"
"buffer_load_dwordx2 v[104:105], off, s[52:55], s79 sc1\n"
"buffer_load_dwordx2 v[106:107], off, s[52:55], s64 sc1\n"
"L_selchk_%=:\n"
"s_waitcnt vmcnt(0)\n"
"v_xor_b32 v18, s26, v104\n"
"v_xor_b32 v20, s27, v105\n"
"v_and_b32 v21, 0xffffdfff, v20\n"
"v_xor_b32 v106, s26, v106\n"
"v_xor_b32 v107, s27, v107\n"
"v_or3_b32 v18, v18, v21, v106\n"
"v_or_b32 v18, v18, v107\n"
"v_cmp_ne_u32 vcc, 0, v18\n"
"s_cbranch_vccnz L_sel_%=\n"
"L_kv0ok_%=:\n"
"v_readfirstlane_b32 s79, v20\n"
"v_add_u32 v2, v2, v20\n"
"s_add_u32 s28, s28, s79\n"
"s_add_u32 s84, s84, s79\n"
"s_add_u32 s29, s29, s79\n"
"s_add_u32 s30, s30, s79\n"
"v_mov_b32 v32, v96\n"
"v_mov_b32 v48, v100\n"
"v_mov_b32 v33, v97\n"
"v_mov_b32 v49, v101\n"
"v_mov_b32 v34, v98\n"
"v_mov_b32 v50, v102\n"
"v_mov_b32 v35, v99\n"
"v_mov_b32 v51, v103\n"
"s_waitcnt lgkmcnt(0)\n"
"s_barrier\n"
"s_lshl_b32 s81, s48, 11\n"
"s_add_u32 s81, s81, 16384\n"
"s_mov_b32 m0, s81\n"
"s_nop 0\n"
"buffer_load_dwordx4 v7, s[52:55], s31 offen sc1 lds\n"
"buffer_load_dwordx4 v7, s[52:55], s31 offen offset:1024 sc1 lds\n"
"s_add_u32 m0, s81, 16384\n"
"s_nop 0\n"
"buffer_load_dwordx4 v7, s[52:55], s32 offen sc1 lds\n"
"buffer_load_dwordx4 v7, s[52:55], s32 offen offset:1024 sc1 lds\n"
"s_add_u32 s31, s31, 0x40000\n"
"s_add_u32 s32, s32, 0x40000\n"
"s_xor_b32 s81, s81, 8192\n"
"s_sub_u32 s33, s33, 0x20000\n"
"v_mov_b32 v120, v0\n"
"v_mov_b32 v121, v7\n"
"v_add_u32 v3, -16, v4\n"
"v_mov_b32 v12, 0x3f803f80\n"
"v_mov_b32 v13, 0x3f803f80\n"
"v_mov_b32 v14, 0x3f803f80\n"
"v_mov_b32 v15, 0x3f803f80\n"
"v_cndmask_b32 v16, 0, v12, s[88:89]\n"
"v_cndmask_b32 v17, 0, v12, s[88:89]\n"
"v_mov_b32 v80, 0\n"
"v_mov_b32 v81, 0\n"
"v_mov_b32 v82, 0\n"
"v_mov_b32 v83, 0\n"
"v_mov_b32 v84, 0\n"
"v_mov_b32 v85, 0\n"
"v_mov_b32 v86, 0\n"
"v_mov_b32 v87, 0\n"
"v_mov_b32 v88, 0\n"
"v_mov_b32 v89, 0\n"
"v_mov_b32 v90, 0\n"
"v_mov_b32 v91, 0\n"
"v_mov_b32 v92, 0\n"
"v_mov_b32 v93, 0\n"
"v_mov_b32 v94, 0\n"
"v_mov_b32 v95, 0\n"
"v_mov_b32 v96, 0\n"
"v_mov_b32 v97, 0\n"
"v_mov_b32 v98, 0\n"
"v_mov_b32 v99, 0\n"
"v_mov_b32 v100, 0\n"
"v_mov_b32 v101, 0\n"
"v_mov_b32 v102, 0\n"
"v_mov_b32 v103, 0\n"
"s_mov_b32 s69, -1\n"
"ds_read_b128 v[104:107], v11 offset:8192\n"
"ds_read_b128 v[108:111], v11 offset:9216\n"
"ds_read_b128 v[112:115], v11 offset:10240\n"
"ds_read_b128 v[116:119], v11 offset:11264\n"
"ds_read_b128 v[64:67], v11 offset:12288\n"
"ds_read_b128 v[68:71], v11 offset:13312\n"
"ds_read_b128 v[72:75], v11 offset:14336\n"
"ds_read_b128 v[76:79], v11 offset:15360\n"
"s_waitcnt lgkmcnt(0)\n"
"ds_write_b128 v123, v[32:35] offset:0\n"
"v_mov_b32 v18, 0\n"
"s_branch L_entry_%=\n"
"L_retryA_%=:\n"
"s_add_u32 s75, s75, 1\n"
"s_cmp_ge_u32 s75, 400000\n"
"s_cbranch_scc1 L_exit_%=\n"
"buffer_load_dwordx4 v[32:35], v0, s[52:55], s28 offen sc1\n"
"s_waitcnt vmcnt(0)\n"
"s_branch L_chkA_%=\n"
"L_retryR_%=:\n"
"s_add_u32 s71, s71, 0x10000\n"
"s_add_u32 s75, s75, 1\n"
"s_cmp_ge_u32 s75, 400000\n"
"s_cbranch_scc1 L_exit_%=\n"
"buffer_load_dwordx4 v[48:51], v0, s[52:55], s84 offen sc1\n"
"buffer_load_dword v18, off, s[52:55], s80 sc1\n"
"s_branch L_chkR_%=\n"
"L_retryB_%=:\n"
"s_add_u32 s75, s75, 1\n"
"s_cmp_lt_u32 s75, 400000\n"
"s_cbranch_scc1 L_pollB_%=\n"
"s_branch L_exit_%=\n"
".p2align 6\n"
"L_top_%=:\n"
"s_mov_b32 s79, s50\n"
".p2align 3\n"
"L_dA_%=:\n"
"s_sub_u32 s79, s79, 1\n"
"s_cbranch_scc0 L_dA_%=\n"
"buffer_load_dwordx4 v[32:35], v0, s[52:55], s28 offen sc1\n"
"s_mov_b32 s79, s85\n"
".p2align 3\n"
"L_dA2_%=:\n"
"s_sub_u32 s79, s79, 1\n"
"s_cbranch_scc0 L_dA2_%=\n"
"buffer_load_dwordx4 v[48:51], v0, s[52:55], s84 offen sc1\n"
"buffer_load_dword v18, off, s[52:55], s80 sc1\n"
"s_waitcnt vmcnt(2)\n"
"L_chkA_%=:\n"
"ds_write_b128 v123, v[32:35] offset:0\n"
"v_max3_u32 v19, v32, v33, v34\n"
"v_max_u32 v19, v19, v35\n"
"v_cmp_eq_u32 vcc, -1, v19\n"
"s_cbranch_vccnz L_retryA_%=\n"
"L_entry_%=:\n"
"v_smfmac_f32_16x16x64_bf16 v[80:83], v[32:35], a[0:7], v1\n"
"v_smfmac_f32_16x16x64_bf16 v[84:87], v[32:35], a[128:135], v1\n"
"s_waitcnt lgkmcnt(0)\n"
"s_barrier\n"
"ds_read_b128 v[36:39], v8 offset:0\n"
"ds_read_b128 v[40:43], v9 offset:0\n"
"ds_read_b128 v[44:47], v10 offset:0\n"
"v_smfmac_f32_16x16x64_bf16 v[88:91], v[32:35], a[64:71], v1\n"
"v_smfmac_f32_16x16x64_bf16 v[92:95], v[32:35], a[192:199], v1\n"
"v_smfmac_f32_16x16x64_bf16 v[96:99], v[32:35], v[104:111], v1\n"
"v_smfmac_f32_16x16x64_bf16 v[100:103], v[32:35], v[112:119], v1\n"
"s_waitcnt lgkmcnt(2)\n"
"v_smfmac_f32_16x16x64_bf16 v[80:83], v[36:39], a[8:15], v1\n"
"v_smfmac_f32_16x16x64_bf16 v[84:87], v[36:39], a[136:143], v1\n"
"v_smfmac_f32_16x16x64_bf16 v[88:91], v[36:39], a[72:79], v1\n"
"v_smfmac_f32_16x16x64_bf16 v[92:95], v[36:39], a[200:207], v1\n"
"s_waitcnt lgkmcnt(1)\n"
"v_smfmac_f32_16x16x64_bf16 v[80:83], v[40:43], a[16:23], v1\n"
"v_smfmac_f32_16x16x64_bf16 v[84:87], v[40:43], a[144:151], v1\n"
"v_smfmac_f32_16x16x64_bf16 v[88:91], v[40:43], a[80:87], v1\n"
"v_smfmac_f32_16x16x64_bf16 v[92:95], v[40:43], a[208:215], v1\n"
"L_chkR_%=:\n"
"s_waitcnt vmcnt(0)\n"
"ds_write_b128 v123, v[48:51] offset:4096\n"
"v_max3_u32 v19, v48, v49, v50\n"
"v_max3_u32 v19, v19, v51, v18\n"
"v_cmp_eq_u32 vcc, -1, v19\n"
"s_cbranch_vccnz L_retryR_%=\n"
"s_waitcnt lgkmcnt(1)\n"
"v_smfmac_f32_16x16x64_bf16 v[80:83], v[44:47], a[24:31], v1\n"
"v_smfmac_f32_16x16x64_bf16 v[84:87], v[44:47], a[152:159], v1\n"
"v_mov_b32 v18, s69\n"
"v_smfmac_f32_16x16x64_bf16 v[88:91], v[44:47], a[88:95], v1\n"
"v_smfmac_f32_16x16x64_bf16 v[92:95], v[44:47], a[216:223], v1\n"
"s_waitcnt lgkmcnt(0)\n"
"s_barrier\n"
"ds_read_b128 v[52:55], v8 offset:4096\n"
"ds_read_b128 v[56:59], v9 offset:4096\n"
"ds_read_b128 v[60:63], v10 offset:4096\n"
"v_smfmac_f32_16x16x64_bf16 v[80:83], v[48:51], a[32:39], v1\n"
"v_smfmac_f32_16x16x64_bf16 v[84:87], v[48:51], a[160:167], v1\n"
"s_mov_b64 exec, s[82:83]\n"
"buffer_store_dword v18, off, s[52:55], s35 sc1\n"
"s_mov_b64 exec, -1\n"
"v_smfmac_f32_16x16x64_bf16 v[96:99], v[36:39], v[64:71], v1\n"
"v_smfmac_f32_16x16x64_bf16 v[100:103], v[36:39], v[72:79], v1\n"
"s_waitcnt lgkmcnt(2)\n"
"v_smfmac_f32_16x16x64_bf16 v[80:83], v[52:55], a[40:47], v1\n"
"v_smfmac_f32_16x16x64_bf16 v[84:87], v[52:55], a[168:175], v1\n"
"s_waitcnt lgkmcnt(1)\n"
"v_smfmac_f32_16x16x64_bf16 v[80:83], v[56:59], a[48:55], v1\n"
"v_smfmac_f32_16x16x64_bf16 v[84:87], v[56:59], a[176:183], v1\n"
"s_waitcnt lgkmcnt(0)\n"
"v_smfmac_f32_16x16x64_bf16 v[80:83], v[60:63], a[56:63], v1\n"
"v_smfmac_f32_16x16x64_bf16 v[84:87], v[60:63], a[184:191], v1\n"
"v_smfmac_f32_16x16x64_bf16 v[88:91], v[48:51], a[96:103], v1\n"
"s_mov_b32 m0, s81\n"
"s_nop 0\n"
"buffer_load_dwordx4 v7, s[52:55], s31 offen sc1 lds\n"
"buffer_load_dwordx4 v7, s[52:55], s31 offen offset:1024 sc1 lds\n"
"v_smfmac_f32_16x16x64_bf16 v[92:95], v[48:51], a[224:231], v1\n"
"s_add_u32 m0, s81, 16384\n"
"s_nop 0\n"
"buffer_load_dwordx4 v7, s[52:55], s32 offen sc1 lds\n"
"buffer_load_dwordx4 v7, s[52:55], s32 offen offset:1024 sc1 lds\n"
"v_smfmac_f32_16x16x64_bf16 v[88:91], v[52:55], a[104:111], v1\n"
"buffer_store_dwordx4 v[124:127], v0, s[52:55], s33 offen sc1\n"
"s_add_u32 s33, s33, 0x20000\n"
"s_add_u32 s31, s31, 0x40000\n"
"s_add_u32 s32, s32, 0x40000\n"
"s_xor_b32 s81, s81, 8192\n"
"v_smfmac_f32_16x16x64_bf16 v[92:95], v[52:55], a[232:239], v1\n"
"v_permlane32_swap_b32 v80, v84\n"
"v_permlane32_swap_b32 v81, v85\n"
"v_smfmac_f32_16x16x64_bf16 v[88:91], v[56:59], a[112:119], v1\n"
"v_permlane32_swap_b32 v82, v86\n"
"v_permlane32_swap_b32 v83, v87\n"
"v_smfmac_f32_16x16x64_bf16 v[92:95], v[56:59], a[240:247], v1\n"
"v_add_f32 v80, v80, v84\n"
"v_add_f32 v81, v81, v85\n"
"v_smfmac_f32_16x16x64_bf16 v[88:91], v[60:63], a[120:127], v1\n"
"v_add_f32 v82, v82, v86\n"
"v_add_f32 v83, v83, v87\n"
"v_smfmac_f32_16x16x64_bf16 v[92:95], v[60:63], a[248:255], v1\n"
"v_cvt_pk_bf16_f32 v20, v80, v81\n"
"v_cvt_pk_bf16_f32 v21, v82, v83\n"
"ds_read_b128 v[80:83], v120 offset:16384\n"
"ds_read_b128 v[84:87], v120 offset:20480\n"
"s_nop 3\n"
"v_permlane32_swap_b32 v88, v92\n"
"v_permlane32_swap_b32 v89, v93\n"
"v_permlane32_swap_b32 v90, v94\n"
"v_permlane32_swap_b32 v91, v95\n"
"v_add_f32 v88, v88, v92\n"
"v_add_f32 v89, v89, v93\n"
"v_add_f32 v90, v90, v94\n"
"v_add_f32 v91, v91, v95\n"
"v_cvt_pk_bf16_f32 v22, v88, v89\n"
"v_cvt_pk_bf16_f32 v23, v90, v91\n"
"ds_write2st64_b64 v5, v[20:21], v[22:23] offset1:1\n"
"ds_read_b128 v[88:91], v120 offset:17408\n"
"ds_read_b128 v[92:95], v120 offset:21504\n"
"v_smfmac_f32_16x16x64_bf16 v[96:99], v[40:43], v[144:151], v1\n"
"v_smfmac_f32_16x16x64_bf16 v[100:103], v[40:43], v[208:215], v1\n"
"v_smfmac_f32_16x16x64_bf16 v[96:99], v[44:47], v[152:159], v1\n"
"v_smfmac_f32_16x16x64_bf16 v[100:103], v[44:47], v[216:223], v1\n"
"s_waitcnt lgkmcnt(2)\n"
"s_barrier\n"
"ds_read_b64_tr_b16 v[104:105], v3 offset:0\n"
"ds_read_b64_tr_b16 v[106:107], v3 offset:512\n"
"ds_read_b64_tr_b16 v[108:109], v3 offset:1024\n"
"ds_read_b64_tr_b16 v[110:111], v3 offset:1536\n"
"ds_read_b64_tr_b16 v[112:113], v3 offset:2048\n"
"ds_read_b64_tr_b16 v[114:115], v3 offset:2560\n"
"ds_read_b64_tr_b16 v[116:117], v3 offset:3072\n"
"ds_read_b64_tr_b16 v[118:119], v3 offset:3584\n"
"ds_read_b128 v[32:35], v120 offset:18432\n"
"ds_read_b128 v[36:39], v120 offset:22528\n"
"ds_read_b128 v[40:43], v120 offset:19456\n"
"ds_read_b128 v[44:47], v120 offset:23552\n"
"v_smfmac_f32_16x16x64_bf16 v[96:99], v[48:51], v[160:167], v1\n"
"v_smfmac_f32_16x16x64_bf16 v[100:103], v[48:51], v[224:231], v1\n"
"v_smfmac_f32_16x16x64_bf16 v[96:99], v[52:55], v[168:175], v1\n"
"v_smfmac_f32_16x16x64_bf16 v[100:103], v[52:55], v[232:239], v1\n"
"s_cmp_eq_u32 s72, 63\n"
"s_cbranch_scc1 L_last_%=\n"
"s_waitcnt lgkmcnt(10)\n"
"v_mfma_f32_16x16x32_bf16 v[20:23], v[80:83], v[104:107], 0\n"
"ds_read_b64_tr_b16 v[64:65], v4 offset:0\n"
"ds_read_b64_tr_b16 v[66:67], v4 offset:512\n"
"v_mfma_f32_16x16x32_bf16 v[24:27], v[84:87], v[104:107], 0\n"
"s_waitcnt lgkmcnt(10)\n"
"v_mfma_f32_16x16x32_bf16 v[20:23], v[88:91], v[108:111], v[20:23]\n"
"ds_read_b64_tr_b16 v[68:69], v4 offset:1024\n"
"ds_read_b64_tr_b16 v[70:71], v4 offset:1536\n"
"v_mfma_f32_16x16x32_bf16 v[24:27], v[92:95], v[108:111], v[24:27]\n"
"s_waitcnt lgkmcnt(10)\n"
"s_waitcnt lgkmcnt(6)\n"
"v_mfma_f32_16x16x32_bf16 v[20:23], v[32:35], v[112:115], v[20:23]\n"
"ds_read_b64_tr_b16 v[72:73], v4 offset:2048\n"
"ds_read_b64_tr_b16 v[74:75], v4 offset:2560\n"
"v_mfma_f32_16x16x32_bf16 v[24:27], v[36:39], v[112:115], v[24:27]\n"
"s_waitcnt lgkmcnt(6)\n"
"v_mfma_f32_16x16x32_bf16 v[20:23], v[40:43], v[116:119], v[20:23]\n"
"ds_read_b64_tr_b16 v[76:77], v4 offset:3072\n"
"ds_read_b64_tr_b16 v[78:79], v4 offset:3584\n"
"v_mfma_f32_16x16x32_bf16 v[24:27], v[44:47], v[116:119], v[24:27]\n"
"s_waitcnt lgkmcnt(6)\n"
"v_mfma_f32_16x16x32_bf16 v[28:31], v[64:67], v[104:107], 0\n"
"v_mov_b32 v80, 0\n"
"v_mov_b32 v81, 0\n"
"s_waitcnt lgkmcnt(4)\n"
"v_mfma_f32_16x16x32_bf16 v[28:31], v[68:71], v[108:111], v[28:31]\n"
"v_mov_b32 v82, 0\n"
"v_mov_b32 v83, 0\n"
"s_waitcnt lgkmcnt(2)\n"
"v_mfma_f32_16x16x32_bf16 v[28:31], v[72:75], v[112:115], v[28:31]\n"
"v_max3_f32 v19, v20, v21, v22\n"
"v_max3_f32 v18, v23, v24, v25\n"
"s_waitcnt lgkmcnt(0)\n"
"v_mfma_f32_16x16x32_bf16 v[28:31], v[76:79], v[116:119], v[28:31]\n"
"v_max3_f32 v19, v19, v26, v27\n"
"v_smfmac_f32_16x16x64_bf16 v[96:99], v[56:59], v[176:183], v1\n"
"v_max_f32 v19, v19, v18\n"
"v_cndmask_b32 v20, v24, v20, s[86:87]\n"
"ds_read_b128 v[32:35], v11 offset:0\n"
"ds_read_b128 v[36:39], v11 offset:1024\n"
"v_smfmac_f32_16x16x64_bf16 v[100:103], v[56:59], v[240:247], v1\n"
"v_cndmask_b32 v21, v25, v21, s[86:87]\n"
"ds_read_b128 v[48:51], v121 offset:32768\n"
"ds_read_b128 v[52:55], v121 offset:33792\n"
"v_max3_f32 v19, v19, v28, v29\n"
"v_max3_f32 v19, v19, v30, v31\n"
"v_mov_b32 v18, v19\n"
"v_smfmac_f32_16x16x64_bf16 v[96:99], v[60:63], v[184:191], v1\n"
"v_cndmask_b32 v22, v26, v22, s[86:87]\n"
"v_permlane16_swap_b32 v19, v18\n"
"v_max_f32 v19, v19, v18\n"
"v_mov_b32 v18, v19\n"
"v_smfmac_f32_16x16x64_bf16 v[100:103], v[60:63], v[248:255], v1\n"
"v_cndmask_b32 v23, v27, v23, s[86:87]\n"
"v_permlane32_swap_b32 v19, v18\n"
"v_max_f32 v19, v19, v18\n"
"v_sub_f32 v20, v20, v19\n"
"v_sub_f32 v21, v21, v19\n"
"v_sub_f32 v22, v22, v19\n"
"v_sub_f32 v23, v23, v19\n"
"v_mul_f32 v20, s76, v20\n"
"v_mul_f32 v21, s76, v21\n"
"v_mul_f32 v22, s76, v22\n"
"v_mul_f32 v23, s76, v23\n"
"v_exp_f32 v20, v20\n"
"v_exp_f32 v21, v21\n"
"v_exp_f32 v22, v22\n"
"v_exp_f32 v23, v23\n"
"v_cndmask_b32 v28, v30, v28, s[86:87]\n"
"v_cvt_pk_bf16_f32 v72, v20, v21\n"
"v_cvt_pk_bf16_f32 v73, v22, v23\n"
"v_cndmask_b32 v29, v31, v29, s[86:87]\n"
"v_mov_b32_dpp v74, v72 row_ror:8 row_mask:0xf bank_mask:0xf\n"
"v_mov_b32_dpp v75, v73 row_ror:8 row_mask:0xf bank_mask:0xf\n"
"v_sub_f32 v28, v28, v19\n"
"v_sub_f32 v29, v29, v19\n"
"s_waitcnt lgkmcnt(0)\n"
"v_mfma_f32_16x16x32_bf16 v[20:23], v[48:51], v[72:75], 0\n"
"v_mul_f32 v28, s76, v28\n"
"v_mul_f32 v29, s76, v29\n"
"v_exp_f32 v28, v28\n"
"v_exp_f32 v29, v29\n"
"v_mfma_f32_16x16x32_bf16 v[24:27], v[52:55], v[72:75], 0\n"
"v_cvt_pk_bf16_f32 v76, v28, v29\n"
"v_cvt_pk_bf16_f32 v124, v96, v97\n"
"v_cvt_pk_bf16_f32 v125, v98, v99\n"
"v_mov_b32_dpp v77, v76 row_ror:8 row_mask:0xf bank_mask:0xf\n"
"v_mfma_f32_16x16x32_bf16 v[64:67], v[72:75], v[12:15], 0\n"
"v_cvt_pk_bf16_f32 v126, v100, v101\n"
"v_cvt_pk_bf16_f32 v127, v102, v103\n"
"v_mfma_f32_16x16x16_bf16 v[20:23], v[124:125], v[76:77], v[20:23]\n"
"v_mov_b32 v84, 0\n"
"v_mfma_f32_16x16x16_bf16 v[24:27], v[126:127], v[76:77], v[24:27]\n"
"ds_read_b128 v[40:43], v11 offset:2048\n"
"ds_read_b128 v[44:47], v11 offset:3072\n"
"v_mov_b32 v85, 0\n"
"v_mov_b32 v86, 0\n"
"v_mov_b32 v87, 0\n"
"v_mov_b32 v88, 0\n"
"v_cvt_pk_bf16_f32 v72, v20, v21\n"
"v_cvt_pk_bf16_f32 v73, v22, v23\n"
"v_mfma_f32_16x16x16_bf16 v[64:67], v[76:77], v[16:17], v[64:67]\n"
"v_cvt_pk_bf16_f32 v74, v24, v25\n"
"v_cvt_pk_bf16_f32 v75, v26, v27\n"
"s_mov_b64 exec, s[86:87]\n"
"ds_write2_b64 v6, v[72:73], v[74:75] offset1:32\n"
"s_mov_b64 exec, -1\n"
"v_mov_b32 v89, 0\n"
"v_mov_b32 v90, 0\n"
"v_mov_b32 v91, 0\n"
"v_mov_b32 v92, 0\n"
"v_mov_b32 v93, 0\n"
"v_mov_b32 v94, 0\n"
"v_mov_b32 v95, 0\n"
"v_rcp_f32 v64, v64\n"
"v_rcp_f32 v65, v65\n"
"v_rcp_f32 v66, v66\n"
"v_rcp_f32 v67, v67\n"
"s_waitcnt lgkmcnt(0)\n"
"s_barrier\n"
"ds_read_b128 v[72:75], v0 offset:12288\n"
"ds_read_b128 v[76:79], v0 offset:13312\n"
"ds_read_b128 v[104:107], v11 offset:4096\n"
"ds_read_b128 v[108:111], v11 offset:5120\n"
"ds_read_b128 v[112:115], v11 offset:6144\n"
"ds_read_b128 v[116:119], v11 offset:7168\n"
"v_mul_f32 v64, s74, v64\n"
"v_mul_f32 v66, s74, v66\n"
"s_waitcnt lgkmcnt(5)\n"
"v_smfmac_f32_16x16x64_bf16 v[80:83], v[72:75], v[128:135], v1\n"
"v_smfmac_f32_16x16x64_bf16 v[84:87], v[72:75], v[136:143], v1\n"
"s_waitcnt lgkmcnt(4)\n"
"v_smfmac_f32_16x16x64_bf16 v[80:83], v[76:79], v[192:199], v1\n"
"v_smfmac_f32_16x16x64_bf16 v[84:87], v[76:79], v[200:207], v1\n"
"v_smfmac_f32_16x16x64_bf16 v[88:91], v[72:75], v[32:39], v1\n"
"v_smfmac_f32_16x16x64_bf16 v[92:95], v[72:75], v[40:47], v1\n"
"s_waitcnt lgkmcnt(0)\n"
"s_nop 2\n"
"v_smfmac_f32_16x16x64_bf16 v[88:91], v[76:79], v[104:111], v1\n"
"s_nop 0\n"
"v_permlane32_swap_b32 v80, v84\n"
"v_smfmac_f32_16x16x64_bf16 v[92:95], v[76:79], v[112:119], v1\n"
"v_permlane32_swap_b32 v81, v85\n"
"v_permlane32_swap_b32 v82, v86\n"
"v_permlane32_swap_b32 v83, v87\n"
"v_add_f32 v80, v80, v84\n"
"v_add_f32 v81, v81, v85\n"
"v_add_f32 v82, v82, v86\n"
"v_add_f32 v83, v83, v87\n"
"v_mul_f32 v80, v80, v64\n"
"v_mul_f32 v81, v81, v65\n"
"v_mul_f32 v82, v82, v66\n"
"v_mul_f32 v83, v83, v67\n"
"v_cvt_pk_bf16_f32 v80, v80, v81\n"
"v_cvt_pk_bf16_f32 v82, v82, v83\n"
"v_and_or_b32 v100, v80, -2, s73\n"
"v_and_or_b32 v101, v82, -2, s73\n"
"v_permlane32_swap_b32 v88, v92\n"
"v_permlane32_swap_b32 v89, v93\n"
"v_permlane32_swap_b32 v90, v94\n"
"v_permlane32_swap_b32 v91, v95\n"
"v_add_f32 v88, v88, v92\n"
"v_add_f32 v89, v89, v93\n"
"v_add_f32 v90, v90, v94\n"
"v_add_f32 v91, v91, v95\n"
"v_mul_f32 v88, v88, v64\n"
"v_mul_f32 v89, v89, v65\n"
"v_mul_f32 v90, v90, v66\n"
"v_mul_f32 v91, v91, v67\n"
"v_cvt_pk_bf16_f32 v88, v88, v89\n"
"v_cvt_pk_bf16_f32 v90, v90, v91\n"
"v_and_or_b32 v102, v88, -2, s73\n"
"v_and_or_b32 v103, v90, -2, s73\n"
"s_nop 0\n"
"v_permlane16_swap_b32 v100, v102\n"
"v_permlane16_swap_b32 v101, v103\n"
"buffer_store_dwordx4 v[100:103], v2, s[52:55], 0 offen sc1\n"
"s_cmp_lg_u32 s49, 0\n"
"s_cbranch_scc1 L_pk0_%=\n"
"v_lshl_add_u32 v18, s48, 10, v4\n"
"ds_read_b64_tr_b16 v[124:125], v18\n"
"ds_read_b64_tr_b16 v[126:127], v18 offset:512\n"
"L_pk0_%=:\n"
"ds_read_b128 v[104:107], v11 offset:8192\n"
"ds_read_b128 v[108:111], v11 offset:9216\n"
"ds_read_b128 v[112:115], v11 offset:10240\n"
"ds_read_b128 v[116:119], v11 offset:11264\n"
"ds_read_b128 v[64:67], v11 offset:12288\n"
"ds_read_b128 v[68:71], v11 offset:13312\n"
"ds_read_b128 v[72:75], v11 offset:14336\n"
"ds_read_b128 v[76:79], v11 offset:15360\n"
"v_mov_b32 v80, 0\n"
"v_mov_b32 v81, 0\n"
"v_mov_b32 v82, 0\n"
"v_mov_b32 v83, 0\n"
"v_mov_b32 v84, 0\n"
"v_mov_b32 v85, 0\n"
"v_mov_b32 v86, 0\n"
"v_mov_b32 v87, 0\n"
"v_mov_b32 v88, 0\n"
"v_mov_b32 v89, 0\n"
"v_mov_b32 v90, 0\n"
"v_mov_b32 v91, 0\n"
"v_mov_b32 v92, 0\n"
"v_mov_b32 v93, 0\n"
"v_mov_b32 v94, 0\n"
"v_mov_b32 v95, 0\n"
"v_mov_b32 v96, 0\n"
"v_mov_b32 v97, 0\n"
"v_mov_b32 v98, 0\n"
"v_mov_b32 v99, 0\n"
"v_mov_b32 v100, 0\n"
"v_mov_b32 v101, 0\n"
"v_mov_b32 v102, 0\n"
"v_mov_b32 v103, 0\n"
"v_xor_b32 v120, 0x2000, v120\n"
"v_xor_b32 v121, 0x2000, v121\n"
"s_mov_b32 s69, s72\n"
"s_waitcnt lgkmcnt(0)\n"
"v_min_u32 v124, -2, v124\n"
"v_min_u32 v125, -2, v125\n"
"v_min_u32 v126, -2, v126\n"
"v_min_u32 v127, -2, v127\n"
"s_cmp_lg_u32 s48, 0\n"
"s_cbranch_scc1 L_nored_%=\n"
"v_and_b32 v52, 16, v0\n"
"v_mov_b32 v53, 0x3f80\n"
"v_lshlrev_b32 v53, v52, v53\n"
"v_cndmask_b32 v48, 0, v53, s[90:91]\n"
"v_cndmask_b32 v49, 0, v53, s[92:93]\n"
"v_cndmask_b32 v50, 0, v53, s[94:95]\n"
"v_cndmask_b32 v51, 0, v53, s[96:97]\n"
"v_lshlrev_b32 v52, 3, v52\n"
"v_and_b32 v53, 0x300, v0\n"
"v_add_u32 v52, v52, v53\n"
"v_bfe_u32 v53, v0, 5, 3\n"
"v_lshl_add_u32 v52, v53, 4, v52\n"
"v_lshrrev_b32 v53, 8, v0\n"
"v_and_b32 v54, 0xf0, v0\n"
"v_lshl_add_u32 v53, v53, 2, v54\n"
"s_mov_b32 s79, s51\n"
".p2align 3\n"
"L_dB_%=:\n"
"s_sub_u32 s79, s79, 1\n"
"s_cbranch_scc0 L_dB_%=\n"
"L_pollB_%=:\n"
"buffer_load_dwordx4 v[32:35], v52, s[52:55], s30 offen offset:0 sc1\n"
"buffer_load_dwordx4 v[36:39], v52, s[52:55], s30 offen offset:1024 sc1\n"
"buffer_load_dwordx4 v[40:43], v52, s[52:55], s30 offen offset:2048 sc1\n"
"buffer_load_dwordx4 v[44:47], v52, s[52:55], s30 offen offset:3072 sc1\n"
"L_chkB_%=:\n"
"s_waitcnt vmcnt(3)\n"
"v_mfma_f32_16x16x32_bf16 v[28:31], v[32:35], v[48:51], 0\n"
"v_xor_b32 v18, s73, v32\n"
"v_bitop3_b32 v18, v18, v33, s73 bitop3:0xf6\n"
"v_bitop3_b32 v18, v18, v34, s73 bitop3:0xf6\n"
"v_bitop3_b32 v18, v18, v35, s73 bitop3:0xf6\n"
"s_waitcnt vmcnt(2)\n"
"v_mfma_f32_16x16x32_bf16 v[28:31], v[36:39], v[48:51], v[28:31]\n"
"v_bitop3_b32 v18, v18, v36, s73 bitop3:0xf6\n"
"v_bitop3_b32 v18, v18, v37, s73 bitop3:0xf6\n"
"v_bitop3_b32 v18, v18, v38, s73 bitop3:0xf6\n"
"v_bitop3_b32 v18, v18, v39, s73 bitop3:0xf6\n"
"s_waitcnt vmcnt(1)\n"
"v_mfma_f32_16x16x32_bf16 v[28:31], v[40:43], v[48:51], v[28:31]\n"
"v_bitop3_b32 v18, v18, v40, s73 bitop3:0xf6\n"
"v_bitop3_b32 v18, v18, v41, s73 bitop3:0xf6\n"
"v_bitop3_b32 v18, v18, v42, s73 bitop3:0xf6\n"
"v_bitop3_b32 v18, v18, v43, s73 bitop3:0xf6\n"
"s_waitcnt vmcnt(0)\n"
"v_mfma_f32_16x16x32_bf16 v[28:31], v[44:47], v[48:51], v[28:31]\n"
"v_bitop3_b32 v18, v18, v44, s73 bitop3:0xf6\n"
"v_bitop3_b32 v18, v18, v45, s73 bitop3:0xf6\n"
"v_bitop3_b32 v18, v18, v46, s73 bitop3:0xf6\n"
"v_bitop3_b32 v18, v18, v47, s73 bitop3:0xf6\n"
"v_and_b32 v18, 1, v18\n"
"v_cmp_ne_u32 vcc, 0, v18\n"
"s_cbranch_vccnz L_retryB_%=\n"
"s_nop 0\n"
"v_add_f32 v28, v28, v29\n"
"v_add_f32 v30, v30, v31\n"
"v_cvt_pk_bf16_f32 v28, v28, v30\n"
"v_min_u32 v28, -2, v28\n"
"s_mov_b64 exec, s[86:87]\n"
"buffer_store_dword v28, v53, s[52:55], s29 offen sc1\n"
"s_mov_b64 exec, -1\n"
"s_branch L_bar4_%=\n"
"L_nored_%=:\n"
"s_mov_b32 s79, s70\n"
".p2align 3\n"
"L_dW_%=:\n"
"s_sub_u32 s79, s79, 1\n"
"s_cbranch_scc0 L_dW_%=\n"
"L_bar4_%=:\n"
"s_barrier\n"
"L_red_done_%=:\n"
"s_add_u32 s72, s72, 1\n"
"s_add_u32 s28, s28, 16384\n"
"s_add_u32 s84, s84, 16384\n"
"s_add_u32 s29, s29, 16384\n"
"s_xor_b32 s73, s73, 1\n"
"s_xor_b32 s74, s74, 0xff1079\n"
"s_add_u32 s79, s72, 1\n"
"s_min_u32 s79, s79, 63\n"
"s_lshr_b32 s79, s79, 2\n"
"s_lshl_b32 s79, s79, 7\n"
"s_add_u32 s80, s34, s79\n"
"s_branch L_top_%=\n"
"L_last_%=:\n"
"s_nop 7\n"
"v_lshl_add_u32 v18, s48, 10, v4\n"
"v_smfmac_f32_16x16x64_bf16 v[96:99], v[56:59], v[176:183], v1\n"
"v_smfmac_f32_16x16x64_bf16 v[100:103], v[56:59], v[240:247], v1\n"
"v_smfmac_f32_16x16x64_bf16 v[96:99], v[60:63], v[184:191], v1\n"
"v_smfmac_f32_16x16x64_bf16 v[100:103], v[60:63], v[248:255], v1\n"
"s_nop 6\n"
"v_cvt_pk_bf16_f32 v124, v96, v97\n"
"v_cvt_pk_bf16_f32 v125, v98, v99\n"
"v_cvt_pk_bf16_f32 v126, v100, v101\n"
"v_cvt_pk_bf16_f32 v127, v102, v103\n"
"s_cmp_lg_u32 s49, 0\n"
"s_cbranch_scc1 L_pkl_%=\n"
"ds_read_b64_tr_b16 v[124:125], v18\n"
"ds_read_b64_tr_b16 v[126:127], v18 offset:512\n"
"L_pkl_%=:\n"
"s_waitcnt lgkmcnt(0)\n"
"v_min_u32 v124, -2, v124\n"
"v_min_u32 v125, -2, v125\n"
"v_min_u32 v126, -2, v126\n"
"v_min_u32 v127, -2, v127\n"
"buffer_store_dwordx4 v[124:127], v0, s[52:55], s33 offen sc1\n"
"v_mov_b32 v18, 64\n"
"s_waitcnt vmcnt(0)\n"
"s_barrier\n"
"s_mov_b64 exec, s[82:83]\n"
"buffer_store_dword v18, off, s[52:55], s35 sc1\n"
"s_mov_b64 exec, -1\n"
"L_exit_%=:\n"
"s_waitcnt vmcnt(0) lgkmcnt(0)\n"
      : "+{v[0:15]}"(va), "+{v[16:17]}"(vb), "+{s[36:51]}"(sa), "+{s[28:35]}"(sb)
      :
      : "v18", "v19", "v20", "v21", "v22", "v23", "v24", "v25", "v26", "v27", "v28", "v29", "v30", "v31", "v32", "v33", "v34", "v35", "v36", "v37", "v38",
        "v39", "v40", "v41", "v42", "v43", "v44", "v45", "v46", "v47", "v48", "v49", "v50", "v51", "v52", "v53", "v54", "v55", "v56", "v57", "v58", "v59",
        "v60", "v61", "v62", "v63", "v64", "v65", "v66", "v67", "v68", "v69", "v70", "v71", "v72", "v73", "v74", "v75", "v76", "v77", "v78", "v79", "v80",
        "v81", "v82", "v83", "v84", "v85", "v86", "v87", "v88", "v89", "v90", "v91", "v92", "v93", "v94", "v95", "v96", "v97", "v98", "v99", "v100", "v101",
        "v102", "v103", "v104", "v105", "v106", "v107", "v108", "v109", "v110", "v111", "v112", "v113", "v114", "v115", "v116", "v117", "v118", "v119",
        "v120", "v121", "v123", "v124", "v125", "v126", "v127", "v128", "v129", "v130", "v131", "v132", "v133", "v134", "v135", "v136", "v137", "v138",
        "v139", "v140", "v141", "v142", "v143", "v144", "v145", "v146", "v147", "v148", "v149", "v150", "v151", "v152", "v153", "v154", "v155", "v156",
        "v157", "v158", "v159", "v160", "v161", "v162", "v163", "v164", "v165", "v166", "v167", "v168", "v169", "v170", "v171", "v172", "v173", "v174",
        "v175", "v176", "v177", "v178", "v179", "v180", "v181", "v182", "v183", "v184", "v185", "v186", "v187", "v188", "v189", "v190", "v191", "v192",
        "v193", "v194", "v195", "v196", "v197", "v198", "v199", "v200", "v201", "v202", "v203", "v204", "v205", "v206", "v207", "v208", "v209", "v210",
        "v211", "v212", "v213", "v214", "v215", "v216", "v217", "v218", "v219", "v220", "v221", "v222", "v223", "v224", "v225", "v226", "v227", "v228",
        "v229", "v230", "v231", "v232", "v233", "v234", "v235", "v236", "v237", "v238", "v239", "v240", "v241", "v242", "v243", "v244", "v245", "v246",
        "v247", "v248", "v249", "v250", "v251", "v252", "v253", "v254", "v255", "a0", "a1", "a2", "a3", "a4", "a5", "a6", "a7", "a8", "a9", "a10", "a11",
        "a12", "a13", "a14", "a15", "a16", "a17", "a18", "a19", "a20", "a21", "a22", "a23", "a24", "a25", "a26", "a27", "a28", "a29", "a30", "a31", "a32",
        "a33", "a34", "a35", "a36", "a37", "a38", "a39", "a40", "a41", "a42", "a43", "a44", "a45", "a46", "a47", "a48", "a49", "a50", "a51", "a52", "a53",
        "a54", "a55", "a56", "a57", "a58", "a59", "a60", "a61", "a62", "a63", "a64", "a65", "a66", "a67", "a68", "a69", "a70", "a71", "a72", "a73", "a74",
        "a75", "a76", "a77", "a78", "a79", "a80", "a81", "a82", "a83", "a84", "a85", "a86", "a87", "a88", "a89", "a90", "a91", "a92", "a93", "a94", "a95",
        "a96", "a97", "a98", "a99", "a100", "a101", "a102", "a103", "a104", "a105", "a106", "a107", "a108", "a109", "a110", "a111", "a112", "a113", "a114",
        "a115", "a116", "a117", "a118", "a119", "a120", "a121", "a122", "a123", "a124", "a125", "a126", "a127", "a128", "a129", "a130", "a131", "a132",
        "a133", "a134", "a135", "a136", "a137", "a138", "a139", "a140", "a141", "a142", "a143", "a144", "a145", "a146", "a147", "a148", "a149", "a150",
        "a151", "a152", "a153", "a154", "a155", "a156", "a157", "a158", "a159", "a160", "a161", "a162", "a163", "a164", "a165", "a166", "a167", "a168",
        "a169", "a170", "a171", "a172", "a173", "a174", "a175", "a176", "a177", "a178", "a179", "a180", "a181", "a182", "a183", "a184", "a185", "a186",
        "a187", "a188", "a189", "a190", "a191", "a192", "a193", "a194", "a195", "a196", "a197", "a198", "a199", "a200", "a201", "a202", "a203", "a204",
        "a205", "a206", "a207", "a208", "a209", "a210", "a211", "a212", "a213", "a214", "a215", "a216", "a217", "a218", "a219", "a220", "a221", "a222",
        "a223", "a224", "a225", "a226", "a227", "a228", "a229", "a230", "a231", "a232", "a233", "a234", "a235", "a236", "a237", "a238", "a239", "a240",
        "a241", "a242", "a243", "a244", "a245", "a246", "a247", "a248", "a249", "a250", "a251", "a252", "a253", "a254", "a255", "s52", "s53", "s54", "s55",
        "s56", "s57", "s58", "s59", "s60", "s61", "s62", "s63", "s64", "s65", "s66", "s67", "s68", "s69", "s70", "s71", "s72", "s73", "s74", "s75", "s76",
        "s77", "s78", "s79", "s80", "s81", "s82", "s83", "s84", "s85", "s86", "s87", "s88", "s89", "s90", "s91", "s92", "s93", "s94", "s95", "s96", "s97",
        "s98", "s99", "s22", "s23", "s24", "s25", "s26", "s27", "vcc", "scc", "m0", "memory");
}

__global__ void __launch_bounds__(256, 1) k_main(Args p) {
  extern __shared__ __attribute__((aligned(16))) char smem[];
  if ((blockIdx.x & 7) < 2) chain(p);
  else bulk(p, smem);
}

extern "C" void kernel_launch(void* const* d_in, const int* in_sizes, int n_in, void* d_out, int out_size, void* d_ws, size_t ws_size,
                              hipStream_t stream) {
  Args a;
  a.x = (const u16*)d_in[0]; a.mem0 = (const u16*)d_in[1]; a.Wq = (const u16*)d_in[2]; a.Wk = (const u16*)d_in[3];
  a.Wv = (const u16*)d_in[4]; a.Wo = (const u16*)d_in[5]; a.w_in = (const u16*)d_in[8]; a.w_sp = (const u16*)d_in[9];
  a.b_sp = (const u16*)d_in[10]; a.w_out = (const u16*)d_in[11];
  a.out = (u16*)d_out; a.ws = (char*)d_ws + ((0x200000 - ((size_t)d_ws & 0x1FFFFF)) & 0x1FFFFF);
  a.da = CH_DA; a.db = CH_DB;
  (void)hipFuncSetAttribute((const void*)k_main, hipFuncAttributeMaxDynamicSharedMemorySize, LDS_BYTES);
  k_main<<<256, 256, LDS_BYTES, stream>>>(a);
}
```
